```python
import torch

def _softmax(x, axis=-1):
    return torch.softmax(x, dim=axis)
def _gelu(x):
    return torch.nn.functional.gelu(x, approximate="tanh")
def _silu(x):
    return torch.nn.functional.silu(x)
def _sigmoid(x):
    return torch.sigmoid(x)
def _layernorm(x, axis=-1, eps=1e-5):
    m = x.mean(dim=axis, keepdim=True)
    v = x.var(dim=axis, keepdim=True, unbiased=False)
    return (x - m) / torch.sqrt(v + eps)
def _scatter_add(tgt, idx, src, axis=0):
    return tgt.clone().index_add_(axis, idx, src)
def _segment_sum(src, seg, n):
    out = torch.zeros((n,) + src.shape[1:], dtype=src.dtype)
    return out.index_add_(0, seg, src)
def _pad(x, pw):
    flat = tuple(v for pair in reversed(pw) for v in pair)
    return torch.nn.functional.pad(x, flat)

kernel_name = 'gather_scatter__cheap_equivariant_egnn__graph'

def setup_inputs():

    _w = (lambda a: (lambda t: t.to(torch.bfloat16) if t.is_floating_point() else t)(torch.as_tensor(a)))
    N, deg, D, De = 10000, 64, 64, max(16, 1)
    E = N * deg
    return {
        "h": _w((lambda *s: torch.randn(*s, dtype=torch.bfloat16) * 0.1)(N, D)),
        "pos": _w((lambda *s: torch.randn(*s, dtype=torch.bfloat16) * 0.8)(N, 3)),
        "e_attr": _w((lambda *s: torch.randn(*s, dtype=torch.bfloat16) * 0.8)(E, De)),
        "src": _w((lambda lo, hi, *s: torch.randint(lo, hi, s))(0, N, E).to(torch.int64)),
        "dst": _w((lambda lo, hi, *s: torch.randint(lo, hi, s))(0, N, E).to(torch.int64)),
        "w_e": _w(0.1 * (lambda *s: torch.randn(*s, dtype=torch.bfloat16) * 0.8)(2 * D + 1 + De, D)),
        "w_x": _w(0.1 * (lambda *s: torch.randn(*s, dtype=torch.bfloat16) * 0.1)(D, 1)),
        "w_h": _w(0.1 * (lambda *s: torch.randn(*s, dtype=torch.bfloat16) * 0.1)(2 * D, D)),
    }

def reference(h, pos, e_attr, src, dst, w_e, w_x, w_h):
    N, D = h.shape[0], h.shape[1]
    diff = pos[dst] - pos[src]
    dist2 = torch.sum(diff * diff, axis=-1, keepdims=True)
    e_in = torch.concatenate([h[dst], h[src], dist2, e_attr], axis=-1)
    m_ij = _silu(e_in @ w_e)
    coord_scale = m_ij @ w_x
    pos_new = pos + _segment_sum(diff * coord_scale, dst, N)
    m_i = _segment_sum(m_ij, dst, N)
    h_new = h + _silu(torch.concatenate([h, m_i], axis=-1) @ w_h)
    return h_new, pos_new
```

```cpp
#include <hip/hip_runtime.h>
#include <hip/hip_bf16.h>
#include <stdint.h>

typedef __attribute__((ext_vector_type(8))) short bf16x8;
typedef __attribute__((ext_vector_type(16))) float f32x16;
typedef __attribute__((ext_vector_type(4))) float f32x4;
typedef __attribute__((ext_vector_type(2))) float f32x2;
typedef __attribute__((ext_vector_type(2))) __bf16 bfx2;
typedef __attribute__((ext_vector_type(4))) unsigned u32x4;
typedef __attribute__((ext_vector_type(2))) unsigned u32x2;

#define DI __device__ __forceinline__
#define WAVE_FENCE()                                         \
  do {                                                       \
    __builtin_amdgcn_fence(__ATOMIC_SEQ_CST, "wavefront");   \
    __builtin_amdgcn_wave_barrier();                         \
  } while (0)

static constexpr int D = 64;
static constexpr int DE = 16;
static constexpr int CH = 5120;
static constexpr int SORT_T = 1024;
static constexpr int MAXNB = 2048;
static constexpr int LCAP = 1024;
static constexpr int SROW = 72;

DI float bf2f(unsigned short u) { return __uint_as_float(((unsigned)u) << 16); }
DI unsigned pk_bf16(float a, float b) {
  f32x2 v = {a, b};
  bfx2 r = __builtin_convertvector(v, bfx2);
  return __builtin_bit_cast(unsigned, r);
}
DI float rbf(float a) { return __uint_as_float(pk_bf16(a, a) << 16); }
DI float silu_f(float x) {
  float t = __builtin_amdgcn_exp2f(x * -1.44269504088896340736f);
  return x * __builtin_amdgcn_rcpf(1.0f + t);
}

__global__ __launch_bounds__(SORT_T) void sort_kernel(
    const int* __restrict__ src, const int* __restrict__ dst, int E, int NB, int G,
    uint2* __restrict__ recs, unsigned short* __restrict__ off, int NBP,
    const unsigned short* __restrict__ w_e, const unsigned short* __restrict__ w_h,
    const unsigned short* __restrict__ pos, int N,
    u32x4* __restrict__ wpack, u32x4* __restrict__ whpack, uint2* __restrict__ posP) {
  __shared__ unsigned hist[MAXNB + 32];
  __shared__ unsigned wsum[16];
  __shared__ uint2 stage[CH];
  const int t = threadIdx.x;
  const int b = blockIdx.x;
  if (b >= G) {
    const int pb = b - G;
    if (pb == 0) {
      for (int i = t; i < 20 * 64; i += SORT_T) {
        const int f = i >> 6, l = i & 63;
        const int kap = f >> 1, nu = f & 1;
        const int col = 32 * nu + (l & 31);
        unsigned short v[8];
#pragma unroll
        for (int j = 0; j < 8; ++j) {
          const int kk = 8 * (l >> 5) + j;
          int row;
          if (kap < 8) row = 16 * kap + kk;
          else if (kap == 8) row = 129 + kk;
          else row = (kk == 0) ? 128 : -1;
          v[j] = (row >= 0) ? w_e[row * D + col] : (unsigned short)0;
        }
        u32x4 p;
        p[0] = v[0] | ((unsigned)v[1] << 16);
        p[1] = v[2] | ((unsigned)v[3] << 16);
        p[2] = v[4] | ((unsigned)v[5] << 16);
        p[3] = v[6] | ((unsigned)v[7] << 16);
        wpack[i] = p;
      }
      for (int i = t; i < 16 * 64; i += SORT_T) {
        const int f = i >> 6, l = i & 63;
        const int w = f >> 2, s = f & 3;
        const int col = 16 * w + (l & 15);
        unsigned short v[8];
#pragma unroll
        for (int j = 0; j < 8; ++j) v[j] = w_h[(32 * s + 8 * (l >> 4) + j) * D + col];
        u32x4 p;
        p[0] = v[0] | ((unsigned)v[1] << 16);
        p[1] = v[2] | ((unsigned)v[3] << 16);
        p[2] = v[4] | ((unsigned)v[5] << 16);
        p[3] = v[6] | ((unsigned)v[7] << 16);
        whpack[i] = p;
      }
    } else {
      for (int n = t; n < N; n += SORT_T) {
        uint2 p;
        p.x = pos[n * 3] | ((unsigned)pos[n * 3 + 1] << 16);
        p.y = pos[n * 3 + 2];
        posP[n] = p;
      }
    }
    return;
  }
  const int e0 = b * CH;
  const int len = min(CH, E - e0);
  for (int i = t; i < NB + 1; i += SORT_T) hist[i] = 0;
  __syncthreads();
  int dv[CH / SORT_T], sv[CH / SORT_T];
#pragma unroll
  for (int k = 0; k < CH / SORT_T; ++k) {
    const int i = t + k * SORT_T;
    if (i < len) {
      dv[k] = dst[e0 + i];
      sv[k] = src[e0 + i];
    } else {
      dv[k] = -1;
      sv[k] = 0;
    }
  }
#pragma unroll
  for (int k = 0; k < CH / SORT_T; ++k)
    if (dv[k] >= 0) atomicAdd(&hist[dv[k] >> 3], 1u);
  __syncthreads();
  {
    const int i0 = 2 * t;
    const unsigned a = (i0 < NB) ? hist[i0] : 0u;
    const unsigned c = (i0 + 1 < NB) ? hist[i0 + 1] : 0u;
    unsigned s = a + c;
    const int lane = t & 63, wv = t >> 6;
    unsigned incl = s;
#pragma unroll
    for (int d = 1; d < 64; d <<= 1) {
      unsigned o = __shfl_up(incl, d);
      if (lane >= d) incl += o;
    }
    if (lane == 63) wsum[wv] = incl;
    __syncthreads();
    unsigned base = 0;
    for (int w = 0; w < wv; ++w) base += wsum[w];
    const unsigned ex = base + incl - s;
    if (i0 < NB) hist[i0] = ex;
    if (i0 + 1 < NB) hist[i0 + 1] = ex + a;
    if (t == 0) hist[NB] = (unsigned)len;
  }
  __syncthreads();
  for (int i = t; i < NB + 1; i += SORT_T) off[(size_t)b * NBP + i] = (unsigned short)hist[i];
  __syncthreads();
#pragma unroll
  for (int k = 0; k < CH / SORT_T; ++k) {
    if (dv[k] >= 0) {
      const unsigned p = atomicAdd(&hist[dv[k] >> 3], 1u);
      uint2 r;
      r.x = (unsigned)(e0 + t + k * SORT_T);
      r.y = (unsigned)sv[k] | ((unsigned)(dv[k] & 7) << 28);
      stage[p] = r;
    }
  }
  __syncthreads();
  for (int i = t; i < len; i += SORT_T) recs[e0 + i] = stage[i];
}

__global__ __launch_bounds__(256, 3) void main_kernel(
    const unsigned short* __restrict__ h, const unsigned short* __restrict__ e_attr,
    const unsigned short* __restrict__ w_x, const uint2* __restrict__ recs,
    const unsigned short* __restrict__ off, int NBP, int G, int NB, int N,
    const u32x4* __restrict__ wpack, const u32x4* __restrict__ whpack,
    const uint2* __restrict__ posP, unsigned short* __restrict__ out_h,
    unsigned short* __restrict__ out_pos) {
  __shared__ u32x4 wl[20 * 64];
  __shared__ uint2 list[LCAP];
  __shared__ __attribute__((aligned(16))) char Sm[4][32 * SROW];
  __shared__ __attribute__((aligned(16))) unsigned short cat[8][136];
  __shared__ float posd[8][4];
  __shared__ unsigned wtot[4];

  const int t = threadIdx.x, lane = t & 63, wv = t >> 6;
  const int r = lane & 31, hh = lane >> 5;
  const int per = (NB + 7) >> 3;
  const int beta = (blockIdx.x & 7) * per + (blockIdx.x >> 3);
  if (beta >= NB || (int)(blockIdx.x >> 3) >= per) return;
  const int n0 = beta * 8;

  unsigned o0 = 0, o1 = 0;
  if (t < G) {
    o0 = off[(size_t)t * NBP + beta];
    o1 = off[(size_t)t * NBP + beta + 1];
  }
#pragma unroll
  for (int i = 0; i < 5; ++i) wl[t + 256 * i] = wpack[t + 256 * i];
  if (t < 64) {
    const int row = t >> 3, c8 = t & 7;
    const int n = min(n0 + row, N - 1);
    *(u32x4*)&cat[row][c8 * 8] = *(const u32x4*)&h[(size_t)n * D + c8 * 8];
  } else if (t < 72) {
    const int row = t - 64;
    const int n = min(n0 + row, N - 1);
    const uint2 p = posP[n];
    posd[row][0] = bf2f(p.x & 0xffff);
    posd[row][1] = bf2f(p.x >> 16);
    posd[row][2] = bf2f(p.y & 0xffff);
    posd[row][3] = 0.f;
  }
  for (int i = t; i < 4 * 32 * SROW / 4; i += 256) ((unsigned*)Sm)[i] = 0;

  f32x16 Y0, Y1;
#pragma unroll
  for (int i = 0; i < 16; ++i) { Y0[i] = 0.f; Y1[i] = 0.f; }

  unsigned rem = o1 - o0;
  unsigned rptr = (unsigned)t * CH + o0;
  char* Sw = Sm[wv];

  while (true) {
    unsigned incl = rem;
#pragma unroll
    for (int d = 1; d < 64; d <<= 1) {
      unsigned o = __shfl_up(incl, d);
      if (lane >= d) incl += o;
    }
    if (lane == 63) wtot[wv] = incl;
    __syncthreads();
    unsigned base = 0, total = 0;
#pragma unroll
    for (int w = 0; w < 4; ++w) {
      const unsigned x = wtot[w];
      if (w < wv) base += x;
      total += x;
    }
    if (total == 0) break;
    const unsigned pre = base + incl - rem;
    unsigned take = 0;
    if (pre < (unsigned)LCAP) take = min(rem, (unsigned)LCAP - pre);
    for (unsigned i = 0; i < take; ++i) list[pre + i] = recs[rptr + i];
    rem -= take;
    rptr += take;
    const int cnt = (int)min(total, (unsigned)LCAP);
    __syncthreads();

    const int ntiles = (cnt + 31) >> 5;
    for (int tile = wv; tile < ntiles; tile += 4) {
      const int idx = tile * 32 + r;
      const bool valid = idx < cnt;
      const uint2 rc = list[valid ? idx : cnt - 1];
      const unsigned e = rc.x, s = rc.y & 0x0FFFFFFFu, dl = rc.y >> 28;
      const u32x4* hs_p = (const u32x4*)(h + (size_t)s * D + hh * 8);
      u32x4 hs0 = hs_p[0], hs1 = hs_p[2], hs2 = hs_p[4], hs3 = hs_p[6];
      u32x4 ea = *(const u32x4*)(e_attr + (size_t)e * DE + hh * 8);
      const uint2 ps = posP[s];
      const u32x4* hd_p = (const u32x4*)(&cat[dl][hh * 8]);
      u32x4 hd0 = hd_p[0], hd1 = hd_p[2], hd2 = hd_p[4], hd3 = hd_p[6];
      const float dx = rbf(posd[dl][0] - bf2f(ps.x & 0xffff));
      const float dy = rbf(posd[dl][1] - bf2f(ps.x >> 16));
      const float dz = rbf(posd[dl][2] - bf2f(ps.y & 0xffff));
      const float d2 = rbf(rbf(dx * dx) + rbf(dy * dy) + rbf(dz * dz));
      unsigned short* s_a = (unsigned short*)(Sw + (hh * 16 + dl) * SROW) + r;
      unsigned short* s_b = (unsigned short*)(Sw + (hh * 16 + 8 + dl) * SROW) + r;
      if (valid) {
        const unsigned ca = hh ? (__float_as_uint(dy) >> 16) : 0x3f80u;
        const unsigned cb = hh ? (__float_as_uint(dz) >> 16) : (__float_as_uint(dx) >> 16);
        *s_a = (unsigned short)ca;
        *s_b = (unsigned short)cb;
      }
      u32x4 dd;
      dd[0] = hh ? 0u : (__float_as_uint(d2) >> 16);
      dd[1] = 0; dd[2] = 0; dd[3] = 0;

      f32x16 a0, a1;
#pragma unroll
      for (int i = 0; i < 16; ++i) { a0[i] = 0.f; a1[i] = 0.f; }
#define STEP(K, AV)                                                                                  \
  a0 = __builtin_amdgcn_mfma_f32_32x32x16_bf16(__builtin_bit_cast(bf16x8, AV),                     \
                                               __builtin_bit_cast(bf16x8, wl[(2 * K) * 64 + lane]), \
                                               a0, 0, 0, 0);                                        \
  a1 = __builtin_amdgcn_mfma_f32_32x32x16_bf16(__builtin_bit_cast(bf16x8, AV),                     \
                                               __builtin_bit_cast(bf16x8, wl[(2 * K + 1) * 64 + lane]), \
                                               a1, 0, 0, 0);
      STEP(0, hd0) STEP(1, hd1) STEP(2, hd2) STEP(3, hd3)
      STEP(9, dd)
      STEP(8, ea)
      STEP(4, hs0) STEP(5, hs1) STEP(6, hs2) STEP(7, hs3)
#undef STEP
      u32x4 m00, m01, m10, m11;
#pragma unroll
      for (int i = 0; i < 4; ++i) {
        m00[i] = pk_bf16(silu_f(rbf(a0[2 * i])), silu_f(rbf(a0[2 * i + 1])));
        m01[i] = pk_bf16(silu_f(rbf(a0[8 + 2 * i])), silu_f(rbf(a0[8 + 2 * i + 1])));
        m10[i] = pk_bf16(silu_f(rbf(a1[2 * i])), silu_f(rbf(a1[2 * i + 1])));
        m11[i] = pk_bf16(silu_f(rbf(a1[8 + 2 * i])), silu_f(rbf(a1[8 + 2 * i + 1])));
      }
      WAVE_FENCE();
      const char* srow = Sw + r * SROW + hh * 8;
      const u32x2 f00 = *(const u32x2*)(srow), f01 = *(const u32x2*)(srow + 16);
      const u32x2 f10 = *(const u32x2*)(srow + 32), f11 = *(const u32x2*)(srow + 48);
      u32x4 sf0 = {f00[0], f00[1], f01[0], f01[1]};
      u32x4 sf1 = {f10[0], f10[1], f11[0], f11[1]};
      Y0 = __builtin_amdgcn_mfma_f32_32x32x16_bf16(__builtin_bit_cast(bf16x8, sf0), __builtin_bit_cast(bf16x8, m00), Y0, 0, 0, 0);
      Y1 = __builtin_amdgcn_mfma_f32_32x32x16_bf16(__builtin_bit_cast(bf16x8, sf0), __builtin_bit_cast(bf16x8, m10), Y1, 0, 0, 0);
      Y0 = __builtin_amdgcn_mfma_f32_32x32x16_bf16(__builtin_bit_cast(bf16x8, sf1), __builtin_bit_cast(bf16x8, m01), Y0, 0, 0, 0);
      Y1 = __builtin_amdgcn_mfma_f32_32x32x16_bf16(__builtin_bit_cast(bf16x8, sf1), __builtin_bit_cast(bf16x8, m11), Y1, 0, 0, 0);
      WAVE_FENCE();
      if (valid) {
        *s_a = 0;
        *s_b = 0;
      }
    }
    __syncthreads();
    if (total <= (unsigned)LCAP) break;
  }

  float* Yred = (float*)list;
  for (int i = t; i < 32 * 64; i += 256) Yred[i] = 0.f;
  __syncthreads();
#pragma unroll
  for (int i = 0; i < 16; ++i) {
    const int row = (i & 3) + 8 * (i >> 2) + 4 * hh;
    atomicAdd(&Yred[row * 64 + r], Y0[i]);
    atomicAdd(&Yred[row * 64 + 32 + r], Y1[i]);
  }
  __syncthreads();
  {
    const int row = t >> 5, c2 = (t & 31) * 2;
    *(unsigned*)&cat[row][64 + c2] = pk_bf16(Yred[row * 64 + c2], Yred[row * 64 + c2 + 1]);
  }
  if (t < 192) {
    const int pr = t >> 3, part = t & 7;
    const float* yr = &Yred[(8 + pr) * 64 + part * 8];
    float sacc = 0.f;
#pragma unroll
    for (int c = 0; c < 8; ++c) sacc += yr[c] * bf2f(w_x[part * 8 + c]);
    sacc += __shfl_xor(sacc, 1);
    sacc += __shfl_xor(sacc, 2);
    sacc += __shfl_xor(sacc, 4);
    const int node = pr & 7, d = pr >> 3;
    if (part == 0 && n0 + node < N) {
      const float v = posd[node][d] + rbf(sacc);
      out_pos[(size_t)(n0 + node) * 3 + d] = (unsigned short)(pk_bf16(v, v) & 0xffff);
    }
  }
  __syncthreads();
  {
    f32x4 acc = {0.f, 0.f, 0.f, 0.f};
    const int arow = lane & 7, kq = lane >> 4;
#pragma unroll
    for (int s = 0; s < 4; ++s) {
      const u32x4 af = *(const u32x4*)&cat[arow][32 * s + 8 * kq];
      const u32x4 bfr = whpack[(wv * 4 + s) * 64 + lane];
      acc = __builtin_amdgcn_mfma_f32_16x16x32_bf16(__builtin_bit_cast(bf16x8, af), __builtin_bit_cast(bf16x8, bfr), acc, 0, 0, 0);
    }
    if (lane < 32) {
      const int col = 16 * wv + (lane & 15);
#pragma unroll
      for (int i = 0; i < 4; ++i) {
        const int row = (lane >> 4) * 4 + i;
        if (n0 + row < N) {
          const float u = rbf(silu_f(rbf(acc[i])));
          const float v = bf2f(cat[row][col]) + u;
          out_h[(size_t)(n0 + row) * D + col] = (unsigned short)(pk_bf16(v, v) & 0xffff);
        }
      }
    }
  }
}

extern "C" void kernel_launch(void* const* d_in, const int* in_sizes, int n_in, void* d_out,
                              int out_size, void* d_ws, size_t ws_size, hipStream_t stream) {
  const unsigned short* h = (const unsigned short*)d_in[0];
  const unsigned short* pos = (const unsigned short*)d_in[1];
  const unsigned short* e_attr = (const unsigned short*)d_in[2];
  const int* src = (const int*)d_in[3];
  const int* dst = (const int*)d_in[4];
  const unsigned short* w_e = (const unsigned short*)d_in[5];
  const unsigned short* w_x = (const unsigned short*)d_in[6];
  const unsigned short* w_h = (const unsigned short*)d_in[7];
  const int N = in_sizes[0] / D;
  const int E = in_sizes[3];
  const int NB = (N + 7) / 8;
  const int G = (E + CH - 1) / CH;
  const int NBP = (NB + 2) & ~1;

  char* ws = (char*)d_ws;
  size_t o = 0;
  uint2* recs = (uint2*)(ws + o); o += (size_t)E * 8; o = (o + 255) & ~(size_t)255;
  unsigned short* off = (unsigned short*)(ws + o); o += (size_t)G * NBP * 2; o = (o + 255) & ~(size_t)255;
  u32x4* wpack = (u32x4*)(ws + o); o += 20 * 64 * 16;
  u32x4* whpack = (u32x4*)(ws + o); o += 16 * 64 * 16;
  uint2* posP = (uint2*)(ws + o); o += (size_t)N * 8;

  unsigned short* out_h = (unsigned short*)d_out;
  unsigned short* out_pos = out_h + (size_t)N * D;

  sort_kernel<<<G + 2, SORT_T, 0, stream>>>(src, dst, E, NB, G, recs, off, NBP, w_e, w_h, pos, N,
                                           wpack, whpack, posP);
  const int per = (NB + 7) / 8;
  main_kernel<<<per * 8, 256, 0, stream>>>(h, e_attr, w_x, recs, off, NBP, G, NB, N, wpack, whpack,
                                           posP, out_h, out_pos);
}
```

```cpp
#include <hip/hip_runtime.h>
#include <hip/hip_bf16.h>
#include <stdint.h>

typedef __attribute__((ext_vector_type(8))) short bf16x8;
typedef __attribute__((ext_vector_type(16))) float f32x16;
typedef __attribute__((ext_vector_type(4))) float f32x4;
typedef __attribute__((ext_vector_type(2))) float f32x2;
typedef __attribute__((ext_vector_type(2))) __bf16 bfx2;
typedef __attribute__((ext_vector_type(4))) unsigned u32x4;
typedef __attribute__((ext_vector_type(2))) unsigned u32x2;

#define DI __device__ __forceinline__
#define WAVE_FENCE()                                         \
  do {                                                       \
    __builtin_amdgcn_fence(__ATOMIC_SEQ_CST, "wavefront");   \
    __builtin_amdgcn_wave_barrier();                         \
  } while (0)

static constexpr int D = 64;
static constexpr int DE = 16;
static constexpr int CH = 5120;
static constexpr int SORT_T = 1024;
static constexpr int MAXNB = 2048;
static constexpr int LCAP = 1024;
static constexpr int SROW = 72;

DI float bf2f(unsigned short u) { return __uint_as_float(((unsigned)u) << 16); }
DI unsigned pk_bf16(float a, float b) {
  f32x2 v = {a, b};
  bfx2 r = __builtin_convertvector(v, bfx2);
  return __builtin_bit_cast(unsigned, r);
}
DI float rbf(float a) { return __uint_as_float(pk_bf16(a, a) << 16); }
DI float silu_f(float x) {
  float t = __builtin_amdgcn_exp2f(x * -1.44269504088896340736f);
  return x * __builtin_amdgcn_rcpf(1.0f + t);
}

__global__ __launch_bounds__(SORT_T) void sort_kernel(
    const int* __restrict__ src, const int* __restrict__ dst, int E, int NB, int G,
    uint2* __restrict__ recs, unsigned short* __restrict__ off, int NBP,
    const unsigned short* __restrict__ w_e, const unsigned short* __restrict__ w_h,
    const unsigned short* __restrict__ pos, int N,
    u32x4* __restrict__ wpack, u32x4* __restrict__ whpack, uint2* __restrict__ posP) {
  __shared__ unsigned hist[MAXNB + 32];
  __shared__ unsigned wsum[16];
  __shared__ uint2 stage[CH];
  const int t = threadIdx.x;
  const int b = blockIdx.x;
  if (b >= G) {
    const int pb = b - G;
    if (pb == 0) {
      for (int i = t; i < 20 * 64; i += SORT_T) {
        const int f = i >> 6, l = i & 63;
        const int kap = f >> 1, nu = f & 1;
        const int col = 32 * nu + (l & 31);
        unsigned short v[8];
#pragma unroll
        for (int j = 0; j < 8; ++j) {
          const int kk = 8 * (l >> 5) + j;
          int row;
          if (kap < 8) row = 16 * kap + kk;
          else if (kap == 8) row = 129 + kk;
          else row = (kk == 0) ? 128 : -1;
          v[j] = (row >= 0) ? w_e[row * D + col] : (unsigned short)0;
        }
        u32x4 p;
        p[0] = v[0] | ((unsigned)v[1] << 16);
        p[1] = v[2] | ((unsigned)v[3] << 16);
        p[2] = v[4] | ((unsigned)v[5] << 16);
        p[3] = v[6] | ((unsigned)v[7] << 16);
        wpack[i] = p;
      }
      for (int i = t; i < 16 * 64; i += SORT_T) {
        const int f = i >> 6, l = i & 63;
        const int w = f >> 2, s = f & 3;
        const int col = 16 * w + (l & 15);
        unsigned short v[8];
#pragma unroll
        for (int j = 0; j < 8; ++j) v[j] = w_h[(32 * s + 8 * (l >> 4) + j) * D + col];
        u32x4 p;
        p[0] = v[0] | ((unsigned)v[1] << 16);
        p[1] = v[2] | ((unsigned)v[3] << 16);
        p[2] = v[4] | ((unsigned)v[5] << 16);
        p[3] = v[6] | ((unsigned)v[7] << 16);
        whpack[i] = p;
      }
    } else {
      for (int n = t; n < N; n += SORT_T) {
        uint2 p;
        p.x = pos[n * 3] | ((unsigned)pos[n * 3 + 1] << 16);
        p.y = pos[n * 3 + 2];
        posP[n] = p;
      }
    }
    return;
  }
  const int e0 = b * CH;
  const int len = min(CH, E - e0);
  for (int i = t; i < NB + 1; i += SORT_T) hist[i] = 0;
  __syncthreads();
  int dv[CH / SORT_T], sv[CH / SORT_T];
#pragma unroll
  for (int k = 0; k < CH / SORT_T; ++k) {
    const int i = t + k * SORT_T;
    if (i < len) {
      dv[k] = dst[e0 + i];
      sv[k] = src[e0 + i];
    } else {
      dv[k] = -1;
      sv[k] = 0;
    }
  }
#pragma unroll
  for (int k = 0; k < CH / SORT_T; ++k)
    if (dv[k] >= 0) atomicAdd(&hist[dv[k] >> 3], 1u);
  __syncthreads();
  {
    const int i0 = 2 * t;
    const unsigned a = (i0 < NB) ? hist[i0] : 0u;
    const unsigned c = (i0 + 1 < NB) ? hist[i0 + 1] : 0u;
    unsigned s = a + c;
    const int lane = t & 63, wv = t >> 6;
    unsigned incl = s;
#pragma unroll
    for (int d = 1; d < 64; d <<= 1) {
      unsigned o = __shfl_up(incl, d);
      if (lane >= d) incl += o;
    }
    if (lane == 63) wsum[wv] = incl;
    __syncthreads();
    unsigned base = 0;
    for (int w = 0; w < wv; ++w) base += wsum[w];
    const unsigned ex = base + incl - s;
    if (i0 < NB) hist[i0] = ex;
    if (i0 + 1 < NB) hist[i0 + 1] = ex + a;
    if (t == 0) hist[NB] = (unsigned)len;
  }
  __syncthreads();
  for (int i = t; i < NB + 1; i += SORT_T) off[(size_t)b * NBP + i] = (unsigned short)hist[i];
  __syncthreads();
#pragma unroll
  for (int k = 0; k < CH / SORT_T; ++k) {
    if (dv[k] >= 0) {
      const unsigned p = atomicAdd(&hist[dv[k] >> 3], 1u);
      uint2 r;
      r.x = (unsigned)(e0 + t + k * SORT_T);
      r.y = (unsigned)sv[k] | ((unsigned)(dv[k] & 7) << 28);
      stage[p] = r;
    }
  }
  __syncthreads();
  for (int i = t; i < len; i += SORT_T) recs[e0 + i] = stage[i];
}

__global__ __launch_bounds__(256, 3) void main_kernel(
    const unsigned short* __restrict__ h, const unsigned short* __restrict__ e_attr,
    const unsigned short* __restrict__ w_x, const uint2* __restrict__ recs,
    const unsigned short* __restrict__ off, int NBP, int G, int NB, int N,
    const u32x4* __restrict__ wpack, const u32x4* __restrict__ whpack,
    const uint2* __restrict__ posP, unsigned short* __restrict__ out_h,
    unsigned short* __restrict__ out_pos) {
  __shared__ __attribute__((aligned(16))) char arena[20 * 1024 + LCAP * 8 + 4 * 32 * SROW];
  u32x4* wl = (u32x4*)arena;
  uint2* list = (uint2*)(arena + 20 * 1024);
  char(*Sm)[32 * SROW] = (char(*)[32 * SROW])(arena + 20 * 1024 + LCAP * 8);
  __shared__ __attribute__((aligned(16))) unsigned short cat[8][136];
  __shared__ float posd[8][4];
  __shared__ unsigned wtot[4];

  const int t = threadIdx.x, lane = t & 63, wv = t >> 6;
  const int r = lane & 31, hh = lane >> 5;
  const int per = (NB + 7) >> 3;
  const int beta = (blockIdx.x & 7) * per + (blockIdx.x >> 3);
  if (beta >= NB || (int)(blockIdx.x >> 3) >= per) return;
  const int n0 = beta * 8;

  unsigned o0 = 0, o1 = 0;
  if (t < G) {
    o0 = off[(size_t)t * NBP + beta];
    o1 = off[(size_t)t * NBP + beta + 1];
  }
#pragma unroll
  for (int i = 0; i < 5; ++i) wl[t + 256 * i] = wpack[t + 256 * i];
  if (t < 64) {
    const int row = t >> 3, c8 = t & 7;
    const int n = min(n0 + row, N - 1);
    *(u32x4*)&cat[row][c8 * 8] = *(const u32x4*)&h[(size_t)n * D + c8 * 8];
  } else if (t < 72) {
    const int row = t - 64;
    const int n = min(n0 + row, N - 1);
    const uint2 p = posP[n];
    posd[row][0] = bf2f(p.x & 0xffff);
    posd[row][1] = bf2f(p.x >> 16);
    posd[row][2] = bf2f(p.y & 0xffff);
    posd[row][3] = 0.f;
  }
  for (int i = t; i < 4 * 32 * SROW / 4; i += 256) ((unsigned*)Sm)[i] = 0;

  f32x16 Y0, Y1;
#pragma unroll
  for (int i = 0; i < 16; ++i) { Y0[i] = 0.f; Y1[i] = 0.f; }

  unsigned rem = o1 - o0;
  unsigned rptr = (unsigned)t * CH + o0;
  char* Sw = Sm[wv];

  while (true) {
    unsigned incl = rem;
#pragma unroll
    for (int d = 1; d < 64; d <<= 1) {
      unsigned o = __shfl_up(incl, d);
      if (lane >= d) incl += o;
    }
    if (lane == 63) wtot[wv] = incl;
    __syncthreads();
    unsigned base = 0, total = 0;
#pragma unroll
    for (int w = 0; w < 4; ++w) {
      const unsigned x = wtot[w];
      if (w < wv) base += x;
      total += x;
    }
    if (total == 0) break;
    const unsigned pre = base + incl - rem;
    unsigned take = 0;
    if (pre < (unsigned)LCAP) take = min(rem, (unsigned)LCAP - pre);
    for (unsigned i = 0; i < take; ++i) list[pre + i] = recs[rptr + i];
    rem -= take;
    rptr += take;
    const int cnt = (int)min(total, (unsigned)LCAP);
    __syncthreads();

    const int ntiles = (cnt + 31) >> 5;
    for (int tile = wv; tile < ntiles; tile += 4) {
      const int idx = tile * 32 + r;
      const bool valid = idx < cnt;
      const uint2 rc = list[valid ? idx : cnt - 1];
      const unsigned e = rc.x, s = rc.y & 0x0FFFFFFFu, dl = rc.y >> 28;
      const u32x4* hs_p = (const u32x4*)(h + (size_t)s * D + hh * 8);
      u32x4 hs0 = hs_p[0], hs1 = hs_p[2], hs2 = hs_p[4], hs3 = hs_p[6];
      u32x4 ea = *(const u32x4*)(e_attr + (size_t)e * DE + hh * 8);
      const uint2 ps = posP[s];
      const u32x4* hd_p = (const u32x4*)(&cat[dl][hh * 8]);
      u32x4 hd0 = hd_p[0], hd1 = hd_p[2], hd2 = hd_p[4], hd3 = hd_p[6];
      const float dx = rbf(posd[dl][0] - bf2f(ps.x & 0xffff));
      const float dy = rbf(posd[dl][1] - bf2f(ps.x >> 16));
      const float dz = rbf(posd[dl][2] - bf2f(ps.y & 0xffff));
      const float d2 = rbf(rbf(dx * dx) + rbf(dy * dy) + rbf(dz * dz));
      unsigned short* s_a = (unsigned short*)(Sw + (hh * 16 + dl) * SROW) + r;
      unsigned short* s_b = (unsigned short*)(Sw + (hh * 16 + 8 + dl) * SROW) + r;
      if (valid) {
        const unsigned ca = hh ? (__float_as_uint(dy) >> 16) : 0x3f80u;
        const unsigned cb = hh ? (__float_as_uint(dz) >> 16) : (__float_as_uint(dx) >> 16);
        *s_a = (unsigned short)ca;
        *s_b = (unsigned short)cb;
      }
      u32x4 dd;
      dd[0] = hh ? 0u : (__float_as_uint(d2) >> 16);
      dd[1] = 0; dd[2] = 0; dd[3] = 0;

      f32x16 a0, a1;
#pragma unroll
      for (int i = 0; i < 16; ++i) { a0[i] = 0.f; a1[i] = 0.f; }
#define STEP(K, AV)                                                                                  \
  a0 = __builtin_amdgcn_mfma_f32_32x32x16_bf16(__builtin_bit_cast(bf16x8, AV),                     \
                                               __builtin_bit_cast(bf16x8, wl[(2 * K) * 64 + lane]), \
                                               a0, 0, 0, 0);                                        \
  a1 = __builtin_amdgcn_mfma_f32_32x32x16_bf16(__builtin_bit_cast(bf16x8, AV),                     \
                                               __builtin_bit_cast(bf16x8, wl[(2 * K + 1) * 64 + lane]), \
                                               a1, 0, 0, 0);
      STEP(0, hd0) STEP(1, hd1) STEP(2, hd2) STEP(3, hd3)
      STEP(9, dd)
      STEP(8, ea)
      STEP(4, hs0) STEP(5, hs1) STEP(6, hs2) STEP(7, hs3)
#undef STEP
      u32x4 m00, m01, m10, m11;
#pragma unroll
      for (int i = 0; i < 4; ++i) {
        m00[i] = pk_bf16(silu_f(rbf(a0[2 * i])), silu_f(rbf(a0[2 * i + 1])));
        m01[i] = pk_bf16(silu_f(rbf(a0[8 + 2 * i])), silu_f(rbf(a0[8 + 2 * i + 1])));
        m10[i] = pk_bf16(silu_f(rbf(a1[2 * i])), silu_f(rbf(a1[2 * i + 1])));
        m11[i] = pk_bf16(silu_f(rbf(a1[8 + 2 * i])), silu_f(rbf(a1[8 + 2 * i + 1])));
      }
      WAVE_FENCE();
      const char* srow = Sw + r * SROW + hh * 8;
      const u32x2 f00 = *(const u32x2*)(srow), f01 = *(const u32x2*)(srow + 16);
      const u32x2 f10 = *(const u32x2*)(srow + 32), f11 = *(const u32x2*)(srow + 48);
      u32x4 sf0 = {f00[0], f00[1], f01[0], f01[1]};
      u32x4 sf1 = {f10[0], f10[1], f11[0], f11[1]};
      Y0 = __builtin_amdgcn_mfma_f32_32x32x16_bf16(__builtin_bit_cast(bf16x8, sf0), __builtin_bit_cast(bf16x8, m00), Y0, 0, 0, 0);
      Y1 = __builtin_amdgcn_mfma_f32_32x32x16_bf16(__builtin_bit_cast(bf16x8, sf0), __builtin_bit_cast(bf16x8, m10), Y1, 0, 0, 0);
      Y0 = __builtin_amdgcn_mfma_f32_32x32x16_bf16(__builtin_bit_cast(bf16x8, sf1), __builtin_bit_cast(bf16x8, m01), Y0, 0, 0, 0);
      Y1 = __builtin_amdgcn_mfma_f32_32x32x16_bf16(__builtin_bit_cast(bf16x8, sf1), __builtin_bit_cast(bf16x8, m11), Y1, 0, 0, 0);
      WAVE_FENCE();
      if (valid) {
        *s_a = 0;
        *s_b = 0;
      }
    }
    __syncthreads();
    if (total <= (unsigned)LCAP) break;
  }

  float* Ypart = (float*)arena;
#pragma unroll
  for (int i = 0; i < 16; ++i) {
    const int row = (i & 3) + 8 * (i >> 2) + 4 * hh;
    Ypart[wv * 2048 + row * 64 + r] = Y0[i];
    Ypart[wv * 2048 + row * 64 + 32 + r] = Y1[i];
  }
  __syncthreads();
  float* Yred = Ypart;
  {
    f32x4 a = *(const f32x4*)&Ypart[t * 8], b = *(const f32x4*)&Ypart[t * 8 + 4];
#pragma unroll
    for (int w = 1; w < 4; ++w) {
      a += *(const f32x4*)&Ypart[w * 2048 + t * 8];
      b += *(const f32x4*)&Ypart[w * 2048 + t * 8 + 4];
    }
    *(f32x4*)&Yred[t * 8] = a;
    *(f32x4*)&Yred[t * 8 + 4] = b;
  }
  __syncthreads();
  {
    const int row = t >> 5, c2 = (t & 31) * 2;
    *(unsigned*)&cat[row][64 + c2] = pk_bf16(Yred[row * 64 + c2], Yred[row * 64 + c2 + 1]);
  }
  if (t < 192) {
    const int pr = t >> 3, part = t & 7;
    const float* yr = &Yred[(8 + pr) * 64 + part * 8];
    float sacc = 0.f;
#pragma unroll
    for (int c = 0; c < 8; ++c) sacc += yr[c] * bf2f(w_x[part * 8 + c]);
    sacc += __shfl_xor(sacc, 1);
    sacc += __shfl_xor(sacc, 2);
    sacc += __shfl_xor(sacc, 4);
    const int node = pr & 7, d = pr >> 3;
    if (part == 0 && n0 + node < N) {
      const float v = posd[node][d] + rbf(sacc);
      out_pos[(size_t)(n0 + node) * 3 + d] = (unsigned short)(pk_bf16(v, v) & 0xffff);
    }
  }
  __syncthreads();
  {
    f32x4 acc = {0.f, 0.f, 0.f, 0.f};
    const int arow = lane & 7, kq = lane >> 4;
#pragma unroll
    for (int s = 0; s < 4; ++s) {
      const u32x4 af = *(const u32x4*)&cat[arow][32 * s + 8 * kq];
      const u32x4 bfr = whpack[(wv * 4 + s) * 64 + lane];
      acc = __builtin_amdgcn_mfma_f32_16x16x32_bf16(__builtin_bit_cast(bf16x8, af), __builtin_bit_cast(bf16x8, bfr), acc, 0, 0, 0);
    }
    if (lane < 32) {
      const int col = 16 * wv + (lane & 15);
#pragma unroll
      for (int i = 0; i < 4; ++i) {
        const int row = (lane >> 4) * 4 + i;
        if (n0 + row < N) {
          const float u = rbf(silu_f(rbf(acc[i])));
          const float v = bf2f(cat[row][col]) + u;
          out_h[(size_t)(n0 + row) * D + col] = (unsigned short)(pk_bf16(v, v) & 0xffff);
        }
      }
    }
  }
}

extern "C" void kernel_launch(void* const* d_in, const int* in_sizes, int n_in, void* d_out,
                              int out_size, void* d_ws, size_t ws_size, hipStream_t stream) {
  const unsigned short* h = (const unsigned short*)d_in[0];
  const unsigned short* pos = (const unsigned short*)d_in[1];
  const unsigned short* e_attr = (const unsigned short*)d_in[2];
  const int* src = (const int*)d_in[3];
  const int* dst = (const int*)d_in[4];
  const unsigned short* w_e = (const unsigned short*)d_in[5];
  const unsigned short* w_x = (const unsigned short*)d_in[6];
  const unsigned short* w_h = (const unsigned short*)d_in[7];
  const int N = in_sizes[0] / D;
  const int E = in_sizes[3];
  const int NB = (N + 7) / 8;
  const int G = (E + CH - 1) / CH;
  const int NBP = (NB + 2) & ~1;

  char* ws = (char*)d_ws;
  size_t o = 0;
  uint2* recs = (uint2*)(ws + o); o += (size_t)E * 8; o = (o + 255) & ~(size_t)255;
  unsigned short* off = (unsigned short*)(ws + o); o += (size_t)G * NBP * 2; o = (o + 255) & ~(size_t)255;
  u32x4* wpack = (u32x4*)(ws + o); o += 20 * 64 * 16;
  u32x4* whpack = (u32x4*)(ws + o); o += 16 * 64 * 16;
  uint2* posP = (uint2*)(ws + o); o += (size_t)N * 8;

  unsigned short* out_h = (unsigned short*)d_out;
  unsigned short* out_pos = out_h + (size_t)N * D;

  sort_kernel<<<G + 2, SORT_T, 0, stream>>>(src, dst, E, NB, G, recs, off, NBP, w_e, w_h, pos, N,
                                           wpack, whpack, posP);
  const int per = (NB + 7) / 8;
  main_kernel<<<per * 8, 256, 0, stream>>>(h, e_attr, w_x, recs, off, NBP, G, NB, N, wpack, whpack,
                                           posP, out_h, out_pos);
}
```

```cpp
#include <hip/hip_runtime.h>
#include <hip/hip_bf16.h>
#include <stdint.h>

typedef __attribute__((ext_vector_type(8))) short bf16x8;
typedef __attribute__((ext_vector_type(16))) float f32x16;
typedef __attribute__((ext_vector_type(4))) float f32x4;
typedef __attribute__((ext_vector_type(2))) float f32x2;
typedef __attribute__((ext_vector_type(2))) __bf16 bfx2;
typedef __attribute__((ext_vector_type(4))) unsigned u32x4;
typedef __attribute__((ext_vector_type(2))) unsigned u32x2;

#define DI __device__ __forceinline__
#define WAVE_FENCE()                                         \
  do {                                                       \
    __builtin_amdgcn_fence(__ATOMIC_SEQ_CST, "wavefront");   \
    __builtin_amdgcn_wave_barrier();                         \
  } while (0)

static constexpr int D = 64;
static constexpr int DE = 16;
static constexpr int CH = 5120;
static constexpr int SORT_T = 1024;
static constexpr int MAXNB = 2048;
static constexpr int LCAP = 1024;
static constexpr int SROW = 72;

DI float bf2f(unsigned short u) { return __uint_as_float(((unsigned)u) << 16); }
DI unsigned pk_bf16(float a, float b) {
  f32x2 v = {a, b};
  bfx2 r = __builtin_convertvector(v, bfx2);
  return __builtin_bit_cast(unsigned, r);
}
DI float rbf(float a) { return __uint_as_float(pk_bf16(a, a) << 16); }
DI float silu_f(float x) {
  float t = __builtin_amdgcn_exp2f(x * -1.44269504088896340736f);
  return x * __builtin_amdgcn_rcpf(1.0f + t);
}

__global__ __launch_bounds__(SORT_T) void sort_kernel(
    const int* __restrict__ src, const int* __restrict__ dst, int E, int NB, int G,
    uint2* __restrict__ recs, unsigned short* __restrict__ off, int NBP,
    const unsigned short* __restrict__ w_e, const unsigned short* __restrict__ w_h,
    const unsigned short* __restrict__ pos, int N,
    u32x4* __restrict__ wpack, u32x4* __restrict__ whpack, uint2* __restrict__ posP) {
  __shared__ unsigned hist[MAXNB + 32];
  __shared__ unsigned wsum[16];
  __shared__ uint2 stage[CH];
  const int t = threadIdx.x;
  const int b = blockIdx.x;
  if (b >= G) {
    const int pb = b - G;
    if (pb == 0) {
      for (int i = t; i < 20 * 64; i += SORT_T) {
        const int f = i >> 6, l = i & 63;
        const int kap = f >> 1, nu = f & 1;
        const int col = 32 * nu + (l & 31);
        unsigned short v[8];
#pragma unroll
        for (int j = 0; j < 8; ++j) {
          const int kk = 8 * (l >> 5) + j;
          int row;
          if (kap < 8) row = 16 * kap + kk;
          else if (kap == 8) row = 129 + kk;
          else row = (kk == 0) ? 128 : -1;
          v[j] = (row >= 0) ? w_e[row * D + col] : (unsigned short)0;
        }
        u32x4 p;
        p[0] = v[0] | ((unsigned)v[1] << 16);
        p[1] = v[2] | ((unsigned)v[3] << 16);
        p[2] = v[4] | ((unsigned)v[5] << 16);
        p[3] = v[6] | ((unsigned)v[7] << 16);
        wpack[i] = p;
      }
      for (int i = t; i < 16 * 64; i += SORT_T) {
        const int f = i >> 6, l = i & 63;
        const int w = f >> 2, s = f & 3;
        const int col = 16 * w + (l & 15);
        unsigned short v[8];
#pragma unroll
        for (int j = 0; j < 8; ++j) v[j] = w_h[(32 * s + 8 * (l >> 4) + j) * D + col];
        u32x4 p;
        p[0] = v[0] | ((unsigned)v[1] << 16);
        p[1] = v[2] | ((unsigned)v[3] << 16);
        p[2] = v[4] | ((unsigned)v[5] << 16);
        p[3] = v[6] | ((unsigned)v[7] << 16);
        whpack[i] = p;
      }
    } else {
      for (int n = t; n < N; n += SORT_T) {
        uint2 p;
        p.x = pos[n * 3] | ((unsigned)pos[n * 3 + 1] << 16);
        p.y = pos[n * 3 + 2];
        posP[n] = p;
      }
    }
    return;
  }
  const int e0 = b * CH;
  const int len = min(CH, E - e0);
  for (int i = t; i < NB + 1; i += SORT_T) hist[i] = 0;
  __syncthreads();
  int dv[CH / SORT_T], sv[CH / SORT_T];
#pragma unroll
  for (int k = 0; k < CH / SORT_T; ++k) {
    const int i = t + k * SORT_T;
    if (i < len) {
      dv[k] = dst[e0 + i];
      sv[k] = src[e0 + i];
    } else {
      dv[k] = -1;
      sv[k] = 0;
    }
  }
#pragma unroll
  for (int k = 0; k < CH / SORT_T; ++k)
    if (dv[k] >= 0) atomicAdd(&hist[dv[k] >> 3], 1u);
  __syncthreads();
  {
    const int i0 = 2 * t;
    const unsigned a = (i0 < NB) ? hist[i0] : 0u;
    const unsigned c = (i0 + 1 < NB) ? hist[i0 + 1] : 0u;
    unsigned s = a + c;
    const int lane = t & 63, wv = t >> 6;
    unsigned incl = s;
#pragma unroll
    for (int d = 1; d < 64; d <<= 1) {
      unsigned o = __shfl_up(incl, d);
      if (lane >= d) incl += o;
    }
    if (lane == 63) wsum[wv] = incl;
    __syncthreads();
    unsigned base = 0;
    for (int w = 0; w < wv; ++w) base += wsum[w];
    const unsigned ex = base + incl - s;
    if (i0 < NB) hist[i0] = ex;
    if (i0 + 1 < NB) hist[i0 + 1] = ex + a;
    if (t == 0) hist[NB] = (unsigned)len;
  }
  __syncthreads();
  for (int i = t; i < NB + 1; i += SORT_T) off[(size_t)b * NBP + i] = (unsigned short)hist[i];
  __syncthreads();
#pragma unroll
  for (int k = 0; k < CH / SORT_T; ++k) {
    if (dv[k] >= 0) {
      const unsigned p = atomicAdd(&hist[dv[k] >> 3], 1u);
      uint2 r;
      r.x = (unsigned)(e0 + t + k * SORT_T);
      r.y = (unsigned)sv[k] | ((unsigned)(dv[k] & 7) << 28);
      stage[p] = r;
    }
  }
  __syncthreads();
  for (int i = t; i < len; i += SORT_T) recs[e0 + i] = stage[i];
}

__global__ __launch_bounds__(256, 2) void main_kernel(
    const unsigned short* __restrict__ h, const unsigned short* __restrict__ e_attr,
    const unsigned short* __restrict__ w_x, const uint2* __restrict__ recs,
    const unsigned short* __restrict__ off, int NBP, int G, int NB, int N,
    const u32x4* __restrict__ wpack, const u32x4* __restrict__ whpack,
    const uint2* __restrict__ posP, unsigned short* __restrict__ out_h,
    unsigned short* __restrict__ out_pos) {
  __shared__ __attribute__((aligned(16))) char arena[20 * 1024 + LCAP * 4 + 4 * 32 * SROW];
  u32x4* wl = (u32x4*)arena;
  unsigned* ridx = (unsigned*)(arena + 20 * 1024);
  char(*Sm)[32 * SROW] = (char(*)[32 * SROW])(arena + 20 * 1024 + LCAP * 4);
  __shared__ __attribute__((aligned(16))) unsigned short cat[8][136];
  __shared__ __attribute__((aligned(16))) float posd[8][4];
  __shared__ unsigned wtot[4];

  const int t = threadIdx.x, lane = t & 63, wv = t >> 6;
  const int r = lane & 31, hh = lane >> 5;
  const int per = (NB + 7) >> 3;
  const int beta = (blockIdx.x & 7) * per + (blockIdx.x >> 3);
  if (beta >= NB || (int)(blockIdx.x >> 3) >= per) return;
  const int n0 = beta * 8;

  unsigned o0 = 0, o1 = 0;
  if (t < G) {
    o0 = off[(size_t)t * NBP + beta];
    o1 = off[(size_t)t * NBP + beta + 1];
  }
#pragma unroll
  for (int i = 0; i < 5; ++i) wl[t + 256 * i] = wpack[t + 256 * i];
  if (t < 64) {
    const int row = t >> 3, c8 = t & 7;
    const int n = min(n0 + row, N - 1);
    *(u32x4*)&cat[row][c8 * 8] = *(const u32x4*)&h[(size_t)n * D + c8 * 8];
  } else if (t < 72) {
    const int row = t - 64;
    const int n = min(n0 + row, N - 1);
    const uint2 p = posP[n];
    posd[row][0] = bf2f(p.x & 0xffff);
    posd[row][1] = bf2f(p.x >> 16);
    posd[row][2] = bf2f(p.y & 0xffff);
    posd[row][3] = 0.f;
  }
  for (int i = t; i < 4 * 32 * SROW / 4; i += 256) ((unsigned*)Sm)[i] = 0;

  f32x16 Y0, Y1;
#pragma unroll
  for (int i = 0; i < 16; ++i) { Y0[i] = 0.f; Y1[i] = 0.f; }
  char* Sw = Sm[wv];

  struct Gath { u32x4 hs0, hs1, hs2, hs3, ea; uint2 ps; };

  for (int g0 = 0; g0 < G; g0 += 256) {
    unsigned rem = o1 - o0;
    unsigned rptr = (unsigned)(g0 + t) * CH + o0;
    while (true) {
      unsigned incl = rem;
#pragma unroll
      for (int d = 1; d < 64; d <<= 1) {
        unsigned o = __shfl_up(incl, d);
        if (lane >= d) incl += o;
      }
      if (lane == 63) wtot[wv] = incl;
      __syncthreads();
      unsigned base = 0, total = 0;
#pragma unroll
      for (int w = 0; w < 4; ++w) {
        const unsigned x = wtot[w];
        if (w < wv) base += x;
        total += x;
      }
      if (total == 0) { __syncthreads(); break; }
      const unsigned pre = base + incl - rem;
      unsigned take = 0;
      if (pre < (unsigned)LCAP) take = min(rem, (unsigned)LCAP - pre);
      for (unsigned i = 0; i < take; ++i) ridx[pre + i] = rptr + i;
      rem -= take;
      rptr += take;
      const int cnt = (int)min(total, (unsigned)LCAP);
      __syncthreads();

      const int ntiles = (cnt + 31) >> 5;
      const int nj = (ntiles - wv + 3) >> 2;
      auto ld_rec = [&](int j) -> uint2 {
        const int idx = min((wv + 4 * j) * 32 + r, cnt - 1);
        return recs[ridx[idx]];
      };
      auto gather = [&](const uint2 rc, Gath& g) {
        const unsigned e = rc.x, s = rc.y & 0x0FFFFFFFu;
        const u32x4* hs_p = (const u32x4*)(h + (size_t)s * D + hh * 8);
        g.hs0 = hs_p[0]; g.hs1 = hs_p[2]; g.hs2 = hs_p[4]; g.hs3 = hs_p[6];
        g.ea = *(const u32x4*)(e_attr + (size_t)e * DE + hh * 8);
        g.ps = posP[s];
      };
      auto compute = [&](const uint2 rc, const Gath& g, int j) {
        const bool valid = (wv + 4 * j) * 32 + r < cnt;
        const unsigned dl = rc.y >> 28;
        const u32x4* hd_p = (const u32x4*)(&cat[dl][hh * 8]);
        const u32x4 hd0 = hd_p[0], hd1 = hd_p[2], hd2 = hd_p[4], hd3 = hd_p[6];
        const f32x4 pd = *(const f32x4*)&posd[dl][0];
        const float dx = rbf(pd[0] - bf2f(g.ps.x & 0xffff));
        const float dy = rbf(pd[1] - bf2f(g.ps.x >> 16));
        const float dz = rbf(pd[2] - bf2f(g.ps.y & 0xffff));
        const float d2 = rbf(rbf(dx * dx) + rbf(dy * dy) + rbf(dz * dz));
        unsigned short* s_a = (unsigned short*)(Sw + (hh * 16 + dl) * SROW) + r;
        unsigned short* s_b = (unsigned short*)(Sw + (hh * 16 + 8 + dl) * SROW) + r;
        if (valid) {
          const unsigned ca = hh ? (__float_as_uint(dy) >> 16) : 0x3f80u;
          const unsigned cb = hh ? (__float_as_uint(dz) >> 16) : (__float_as_uint(dx) >> 16);
          *s_a = (unsigned short)ca;
          *s_b = (unsigned short)cb;
        }
        u32x4 dd;
        dd[0] = hh ? 0u : (__float_as_uint(d2) >> 16);
        dd[1] = 0; dd[2] = 0; dd[3] = 0;

        f32x16 a0, a1;
#pragma unroll
        for (int i = 0; i < 16; ++i) { a0[i] = 0.f; a1[i] = 0.f; }
#define STEP(K, AV)                                                                                  \
  a0 = __builtin_amdgcn_mfma_f32_32x32x16_bf16(__builtin_bit_cast(bf16x8, AV),                     \
                                               __builtin_bit_cast(bf16x8, wl[(2 * K) * 64 + lane]), \
                                               a0, 0, 0, 0);                                        \
  a1 = __builtin_amdgcn_mfma_f32_32x32x16_bf16(__builtin_bit_cast(bf16x8, AV),                     \
                                               __builtin_bit_cast(bf16x8, wl[(2 * K + 1) * 64 + lane]), \
                                               a1, 0, 0, 0);
        STEP(0, hd0) STEP(1, hd1) STEP(2, hd2) STEP(3, hd3)
        STEP(9, dd)
        STEP(8, g.ea)
        STEP(4, g.hs0) STEP(5, g.hs1) STEP(6, g.hs2) STEP(7, g.hs3)
#undef STEP
        u32x4 m00, m01, m10, m11;
#pragma unroll
        for (int i = 0; i < 4; ++i) {
          m00[i] = pk_bf16(silu_f(a0[2 * i]), silu_f(a0[2 * i + 1]));
          m01[i] = pk_bf16(silu_f(a0[8 + 2 * i]), silu_f(a0[8 + 2 * i + 1]));
          m10[i] = pk_bf16(silu_f(a1[2 * i]), silu_f(a1[2 * i + 1]));
          m11[i] = pk_bf16(silu_f(a1[8 + 2 * i]), silu_f(a1[8 + 2 * i + 1]));
        }
        WAVE_FENCE();
        const char* srow = Sw + r * SROW + hh * 8;
        const u32x2 f00 = *(const u32x2*)(srow), f01 = *(const u32x2*)(srow + 16);
        const u32x2 f10 = *(const u32x2*)(srow + 32), f11 = *(const u32x2*)(srow + 48);
        u32x4 sf0 = {f00[0], f00[1], f01[0], f01[1]};
        u32x4 sf1 = {f10[0], f10[1], f11[0], f11[1]};
        Y0 = __builtin_amdgcn_mfma_f32_32x32x16_bf16(__builtin_bit_cast(bf16x8, sf0), __builtin_bit_cast(bf16x8, m00), Y0, 0, 0, 0);
        Y1 = __builtin_amdgcn_mfma_f32_32x32x16_bf16(__builtin_bit_cast(bf16x8, sf0), __builtin_bit_cast(bf16x8, m10), Y1, 0, 0, 0);
        Y0 = __builtin_amdgcn_mfma_f32_32x32x16_bf16(__builtin_bit_cast(bf16x8, sf1), __builtin_bit_cast(bf16x8, m01), Y0, 0, 0, 0);
        Y1 = __builtin_amdgcn_mfma_f32_32x32x16_bf16(__builtin_bit_cast(bf16x8, sf1), __builtin_bit_cast(bf16x8, m11), Y1, 0, 0, 0);
        WAVE_FENCE();
        if (valid) {
          *s_a = 0;
          *s_b = 0;
        }
      };

      if (nj > 0) {
        Gath gA, gB;
        uint2 rc0 = ld_rec(0), rc1 = ld_rec(1);
        gather(rc0, gA);
        for (int j = 0; j < nj; j += 2) {
          const uint2 rc2 = ld_rec(j + 2);
          gather(rc1, gB);
          compute(rc0, gA, j);
          if (j + 1 >= nj) break;
          const uint2 rc3 = ld_rec(j + 3);
          gather(rc2, gA);
          compute(rc1, gB, j + 1);
          rc0 = rc2;
          rc1 = rc3;
        }
      }
      __syncthreads();
      if (total <= (unsigned)LCAP) break;
    }
    o0 = 0; o1 = 0;
    if (g0 + 256 + t < G) {
      o0 = off[(size_t)(g0 + 256 + t) * NBP + beta];
      o1 = off[(size_t)(g0 + 256 + t) * NBP + beta + 1];
    }
  }

  float* Ypart = (float*)arena;
#pragma unroll
  for (int i = 0; i < 16; ++i) {
    const int row = (i & 3) + 8 * (i >> 2) + 4 * hh;
    Ypart[wv * 2048 + row * 64 + r] = Y0[i];
    Ypart[wv * 2048 + row * 64 + 32 + r] = Y1[i];
  }
  __syncthreads();
  float* Yred = Ypart;
  {
    f32x4 a = *(const f32x4*)&Ypart[t * 8], b = *(const f32x4*)&Ypart[t * 8 + 4];
#pragma unroll
    for (int w = 1; w < 4; ++w) {
      a += *(const f32x4*)&Ypart[w * 2048 + t * 8];
      b += *(const f32x4*)&Ypart[w * 2048 + t * 8 + 4];
    }
    *(f32x4*)&Yred[t * 8] = a;
    *(f32x4*)&Yred[t * 8 + 4] = b;
  }
  __syncthreads();
  {
    const int row = t >> 5, c2 = (t & 31) * 2;
    *(unsigned*)&cat[row][64 + c2] = pk_bf16(Yred[row * 64 + c2], Yred[row * 64 + c2 + 1]);
  }
  if (t < 192) {
    const int pr = t >> 3, part = t & 7;
    const float* yr = &Yred[(8 + pr) * 64 + part * 8];
    float sacc = 0.f;
#pragma unroll
    for (int c = 0; c < 8; ++c) sacc += yr[c] * bf2f(w_x[part * 8 + c]);
    sacc += __shfl_xor(sacc, 1);
    sacc += __shfl_xor(sacc, 2);
    sacc += __shfl_xor(sacc, 4);
    const int node = pr & 7, d = pr >> 3;
    if (part == 0 && n0 + node < N) {
      const float v = posd[node][d] + rbf(sacc);
      out_pos[(size_t)(n0 + node) * 3 + d] = (unsigned short)(pk_bf16(v, v) & 0xffff);
    }
  }
  __syncthreads();
  {
    f32x4 acc = {0.f, 0.f, 0.f, 0.f};
    const int arow = lane & 7, kq = lane >> 4;
#pragma unroll
    for (int s = 0; s < 4; ++s) {
      const u32x4 af = *(const u32x4*)&cat[arow][32 * s + 8 * kq];
      const u32x4 bfr = whpack[(wv * 4 + s) * 64 + lane];
      acc = __builtin_amdgcn_mfma_f32_16x16x32_bf16(__builtin_bit_cast(bf16x8, af), __builtin_bit_cast(bf16x8, bfr), acc, 0, 0, 0);
    }
    if (lane < 32) {
      const int col = 16 * wv + (lane & 15);
#pragma unroll
      for (int i = 0; i < 4; ++i) {
        const int row = (lane >> 4) * 4 + i;
        if (n0 + row < N) {
          const float u = rbf(silu_f(rbf(acc[i])));
          const float v = bf2f(cat[row][col]) + u;
          out_h[(size_t)(n0 + row) * D + col] = (unsigned short)(pk_bf16(v, v) & 0xffff);
        }
      }
    }
  }
}

extern "C" void kernel_launch(void* const* d_in, const int* in_sizes, int n_in, void* d_out,
                              int out_size, void* d_ws, size_t ws_size, hipStream_t stream) {
  const unsigned short* h = (const unsigned short*)d_in[0];
  const unsigned short* pos = (const unsigned short*)d_in[1];
  const unsigned short* e_attr = (const unsigned short*)d_in[2];
  const int* src = (const int*)d_in[3];
  const int* dst = (const int*)d_in[4];
  const unsigned short* w_e = (const unsigned short*)d_in[5];
  const unsigned short* w_x = (const unsigned short*)d_in[6];
  const unsigned short* w_h = (const unsigned short*)d_in[7];
  const int N = in_sizes[0] / D;
  const int E = in_sizes[3];
  const int NB = (N + 7) / 8;
  const int G = (E + CH - 1) / CH;
  const int NBP = (NB + 2) & ~1;

  char* ws = (char*)d_ws;
  size_t o = 0;
  uint2* recs = (uint2*)(ws + o); o += (size_t)E * 8; o = (o + 255) & ~(size_t)255;
  unsigned short* off = (unsigned short*)(ws + o); o += (size_t)G * NBP * 2; o = (o + 255) & ~(size_t)255;
  u32x4* wpack = (u32x4*)(ws + o); o += 20 * 64 * 16;
  u32x4* whpack = (u32x4*)(ws + o); o += 16 * 64 * 16;
  uint2* posP = (uint2*)(ws + o); o += (size_t)N * 8;

  unsigned short* out_h = (unsigned short*)d_out;
  unsigned short* out_pos = out_h + (size_t)N * D;

  sort_kernel<<<G + 2, SORT_T, 0, stream>>>(src, dst, E, NB, G, recs, off, NBP, w_e, w_h, pos, N,
                                           wpack, whpack, posP);
  const int per = (NB + 7) / 8;
  main_kernel<<<per * 8, 256, 0, stream>>>(h, e_attr, w_x, recs, off, NBP, G, NB, N, wpack, whpack,
                                           posP, out_h, out_pos);
}
```

```cpp
#include <hip/hip_runtime.h>
#include <hip/hip_bf16.h>
#include <stdint.h>

typedef __attribute__((ext_vector_type(8))) short bf16x8;
typedef __attribute__((ext_vector_type(16))) float f32x16;
typedef __attribute__((ext_vector_type(4))) float f32x4;
typedef __attribute__((ext_vector_type(2))) float f32x2;
typedef __attribute__((ext_vector_type(2))) __bf16 bfx2;
typedef __attribute__((ext_vector_type(4))) unsigned u32x4;
typedef __attribute__((ext_vector_type(2))) unsigned u32x2;

#define DI __device__ __forceinline__
#define WAVE_FENCE()                                         \
  do {                                                       \
    __builtin_amdgcn_fence(__ATOMIC_SEQ_CST, "wavefront");   \
    __builtin_amdgcn_wave_barrier();                         \
  } while (0)

static constexpr int D = 64;
static constexpr int DE = 16;
static constexpr int CH = 5120;
static constexpr int SORT_T = 1024;
static constexpr int MAXNB = 2048;
static constexpr int LCAP = 1024;
static constexpr int SROW = 72;

DI float bf2f(unsigned short u) { return __uint_as_float(((unsigned)u) << 16); }
DI unsigned pk_bf16(float a, float b) {
  f32x2 v = {a, b};
  bfx2 r = __builtin_convertvector(v, bfx2);
  return __builtin_bit_cast(unsigned, r);
}
DI float rbf(float a) { return __uint_as_float(pk_bf16(a, a) << 16); }
DI float silu_f(float x) {
  float t = __builtin_amdgcn_exp2f(x * -1.44269504088896340736f);
  return x * __builtin_amdgcn_rcpf(1.0f + t);
}

__global__ __launch_bounds__(SORT_T) void sort_kernel(
    const int* __restrict__ src, const int* __restrict__ dst, int E, int NB, int G,
    uint2* __restrict__ recs, unsigned short* __restrict__ off, int NBP,
    const unsigned short* __restrict__ w_e, const unsigned short* __restrict__ w_h,
    const unsigned short* __restrict__ pos, int N,
    u32x4* __restrict__ wpack, u32x4* __restrict__ whpack, uint2* __restrict__ posP) {
  __shared__ unsigned hist[MAXNB + 32];
  __shared__ unsigned wsum[16];
  __shared__ uint2 stage[CH];
  const int t = threadIdx.x;
  const int b = blockIdx.x;
  if (b >= G) {
    const int pb = b - G;
    if (pb == 0) {
      for (int i = t; i < 20 * 64; i += SORT_T) {
        const int f = i >> 6, l = i & 63;
        const int kap = f >> 1, nu = f & 1;
        const int col = 32 * nu + (l & 31);
        unsigned short v[8];
#pragma unroll
        for (int j = 0; j < 8; ++j) {
          const int kk = 8 * (l >> 5) + j;
          int row;
          if (kap < 8) row = 16 * kap + kk;
          else if (kap == 8) row = 129 + kk;
          else row = (kk == 0) ? 128 : -1;
          v[j] = (row >= 0) ? w_e[row * D + col] : (unsigned short)0;
        }
        u32x4 p;
        p[0] = v[0] | ((unsigned)v[1] << 16);
        p[1] = v[2] | ((unsigned)v[3] << 16);
        p[2] = v[4] | ((unsigned)v[5] << 16);
        p[3] = v[6] | ((unsigned)v[7] << 16);
        wpack[i] = p;
      }
      for (int i = t; i < 16 * 64; i += SORT_T) {
        const int f = i >> 6, l = i & 63;
        const int w = f >> 2, s = f & 3;
        const int col = 16 * w + (l & 15);
        unsigned short v[8];
#pragma unroll
        for (int j = 0; j < 8; ++j) v[j] = w_h[(32 * s + 8 * (l >> 4) + j) * D + col];
        u32x4 p;
        p[0] = v[0] | ((unsigned)v[1] << 16);
        p[1] = v[2] | ((unsigned)v[3] << 16);
        p[2] = v[4] | ((unsigned)v[5] << 16);
        p[3] = v[6] | ((unsigned)v[7] << 16);
        whpack[i] = p;
      }
    } else {
      for (int n = t; n < N; n += SORT_T) {
        uint2 p;
        p.x = pos[n * 3] | ((unsigned)pos[n * 3 + 1] << 16);
        p.y = pos[n * 3 + 2];
        posP[n] = p;
      }
    }
    return;
  }
  const int e0 = b * CH;
  const int len = min(CH, E - e0);
  for (int i = t; i < NB + 1; i += SORT_T) hist[i] = 0;
  __syncthreads();
  int dv[CH / SORT_T], sv[CH / SORT_T];
#pragma unroll
  for (int k = 0; k < CH / SORT_T; ++k) {
    const int i = t + k * SORT_T;
    if (i < len) {
      dv[k] = dst[e0 + i];
      sv[k] = src[e0 + i];
    } else {
      dv[k] = -1;
      sv[k] = 0;
    }
  }
#pragma unroll
  for (int k = 0; k < CH / SORT_T; ++k)
    if (dv[k] >= 0) atomicAdd(&hist[dv[k] >> 3], 1u);
  __syncthreads();
  {
    const int i0 = 2 * t;
    const unsigned a = (i0 < NB) ? hist[i0] : 0u;
    const unsigned c = (i0 + 1 < NB) ? hist[i0 + 1] : 0u;
    unsigned s = a + c;
    const int lane = t & 63, wv = t >> 6;
    unsigned incl = s;
#pragma unroll
    for (int d = 1; d < 64; d <<= 1) {
      unsigned o = __shfl_up(incl, d);
      if (lane >= d) incl += o;
    }
    if (lane == 63) wsum[wv] = incl;
    __syncthreads();
    unsigned base = 0;
    for (int w = 0; w < wv; ++w) base += wsum[w];
    const unsigned ex = base + incl - s;
    if (i0 < NB) hist[i0] = ex;
    if (i0 + 1 < NB) hist[i0 + 1] = ex + a;
    if (t == 0) hist[NB] = (unsigned)len;
  }
  __syncthreads();
  for (int i = t; i < NB + 1; i += SORT_T) off[(size_t)b * NBP + i] = (unsigned short)hist[i];
  __syncthreads();
#pragma unroll
  for (int k = 0; k < CH / SORT_T; ++k) {
    if (dv[k] >= 0) {
      const unsigned p = atomicAdd(&hist[dv[k] >> 3], 1u);
      uint2 r;
      r.x = (unsigned)(e0 + t + k * SORT_T);
      r.y = (unsigned)sv[k] | ((unsigned)(dv[k] & 7) << 28);
      stage[p] = r;
    }
  }
  __syncthreads();
  for (int i = t; i < len; i += SORT_T) recs[e0 + i] = stage[i];
}

__global__ __launch_bounds__(256, 2) void main_kernel(
    const unsigned short* __restrict__ h, const unsigned short* __restrict__ e_attr,
    const unsigned short* __restrict__ w_x, const uint2* __restrict__ recs,
    const unsigned short* __restrict__ off, int NBP, int G, int NB, int N,
    const u32x4* __restrict__ wpack, const u32x4* __restrict__ whpack,
    const uint2* __restrict__ posP, unsigned short* __restrict__ out_h,
    unsigned short* __restrict__ out_pos) {
  __shared__ u32x4 wl[20 * 64];
  __shared__ u32x4 whl[16 * 64];
  __shared__ __attribute__((aligned(16))) char arena[32 * 1024];
  unsigned* ridx = (unsigned*)arena;
  char(*Sm)[32 * SROW] = (char(*)[32 * SROW])(arena + LCAP * 4);
  __shared__ __attribute__((aligned(16))) unsigned short cat[8][136];
  __shared__ __attribute__((aligned(16))) float posd[8][4];
  __shared__ float wxs[64];
  __shared__ unsigned wtot[4];

  const int t = threadIdx.x, lane = t & 63, wv = t >> 6;
  const int r = lane & 31, hh = lane >> 5;
  const int per = (NB + 7) >> 3;
  const int nwx = (int)(gridDim.x >> 3);
  const int xcd = blockIdx.x & 7;
  int bl = blockIdx.x >> 3;
  if (bl >= per || xcd * per + bl >= NB) return;

  unsigned o0 = 0, o1 = 0;
  u32x4 hrow = {0u, 0u, 0u, 0u};
  uint2 prow = {0u, 0u};
  auto prefetch_bucket = [&](int beta_) {
    if (t < G) {
      o0 = off[(size_t)t * NBP + beta_];
      o1 = off[(size_t)t * NBP + beta_ + 1];
    }
    if (t < 64) {
      const int n = min(beta_ * 8 + (t >> 3), N - 1);
      hrow = *(const u32x4*)&h[(size_t)n * D + (t & 7) * 8];
    } else if (t < 72) {
      prow = posP[min(beta_ * 8 + t - 64, N - 1)];
    }
  };
  prefetch_bucket(xcd * per + bl);
#pragma unroll
  for (int i = 0; i < 5; ++i) wl[t + 256 * i] = wpack[t + 256 * i];
#pragma unroll
  for (int i = 0; i < 4; ++i) whl[t + 256 * i] = whpack[t + 256 * i];
  if (t < 64) wxs[t] = bf2f(w_x[t]);
  char* Sw = Sm[wv];

  for (; bl < per && xcd * per + bl < NB; bl += nwx) {
  const int beta = xcd * per + bl;
  const int n0 = beta * 8;
  if (t < 64) {
    *(u32x4*)&cat[t >> 3][(t & 7) * 8] = hrow;
  } else if (t < 72) {
    const int row = t - 64;
    posd[row][0] = bf2f(prow.x & 0xffff);
    posd[row][1] = bf2f(prow.x >> 16);
    posd[row][2] = bf2f(prow.y & 0xffff);
    posd[row][3] = 0.f;
  }
  unsigned c0 = o0, c1 = o1;
  if (bl + nwx < per && beta + nwx < NB) prefetch_bucket(beta + nwx);
  for (int i = t; i < 4 * 32 * SROW / 4; i += 256) ((unsigned*)Sm)[i] = 0;

  f32x16 Y0, Y1;
#pragma unroll
  for (int i = 0; i < 16; ++i) { Y0[i] = 0.f; Y1[i] = 0.f; }

  struct Gath { u32x4 hs0, hs1, hs2, hs3, ea; uint2 ps; };

  for (int g0 = 0; g0 < G; g0 += 256) {
    unsigned rem = c1 - c0;
    unsigned rptr = (unsigned)(g0 + t) * CH + c0;
    while (true) {
      unsigned incl = rem;
#pragma unroll
      for (int d = 1; d < 64; d <<= 1) {
        unsigned o = __shfl_up(incl, d);
        if (lane >= d) incl += o;
      }
      if (lane == 63) wtot[wv] = incl;
      __syncthreads();
      unsigned base = 0, total = 0;
#pragma unroll
      for (int w = 0; w < 4; ++w) {
        const unsigned x = wtot[w];
        if (w < wv) base += x;
        total += x;
      }
      if (total == 0) { __syncthreads(); break; }
      const unsigned pre = base + incl - rem;
      unsigned take = 0;
      if (pre < (unsigned)LCAP) take = min(rem, (unsigned)LCAP - pre);
      for (unsigned i = 0; i < take; ++i) ridx[pre + i] = rptr + i;
      rem -= take;
      rptr += take;
      const int cnt = (int)min(total, (unsigned)LCAP);
      __syncthreads();

      const int ntiles = (cnt + 31) >> 5;
      const int nj = (ntiles - wv + 3) >> 2;
      auto ld_rec = [&](int j) -> uint2 {
        const int idx = min((wv + 4 * j) * 32 + r, cnt - 1);
        return recs[ridx[idx]];
      };
      auto gather = [&](const uint2 rc, Gath& g) {
        const unsigned e = rc.x, s = rc.y & 0x0FFFFFFFu;
        const u32x4* hs_p = (const u32x4*)(h + (size_t)s * D + hh * 8);
        g.hs0 = hs_p[0]; g.hs1 = hs_p[2]; g.hs2 = hs_p[4]; g.hs3 = hs_p[6];
        g.ea = *(const u32x4*)(e_attr + (size_t)e * DE + hh * 8);
        g.ps = posP[s];
      };
      auto compute = [&](const uint2 rc, const Gath& g, int j) {
        const bool valid = (wv + 4 * j) * 32 + r < cnt;
        const unsigned dl = rc.y >> 28;
        const u32x4* hd_p = (const u32x4*)(&cat[dl][hh * 8]);
        const u32x4 hd0 = hd_p[0], hd1 = hd_p[2], hd2 = hd_p[4], hd3 = hd_p[6];
        const f32x4 pd = *(const f32x4*)&posd[dl][0];
        const float dx = rbf(pd[0] - bf2f(g.ps.x & 0xffff));
        const float dy = rbf(pd[1] - bf2f(g.ps.x >> 16));
        const float dz = rbf(pd[2] - bf2f(g.ps.y & 0xffff));
        const float d2 = rbf(rbf(dx * dx) + rbf(dy * dy) + rbf(dz * dz));
        unsigned short* s_a = (unsigned short*)(Sw + (hh * 16 + dl) * SROW) + r;
        unsigned short* s_b = (unsigned short*)(Sw + (hh * 16 + 8 + dl) * SROW) + r;
        if (valid) {
          const unsigned ca = hh ? (__float_as_uint(dy) >> 16) : 0x3f80u;
          const unsigned cb = hh ? (__float_as_uint(dz) >> 16) : (__float_as_uint(dx) >> 16);
          *s_a = (unsigned short)ca;
          *s_b = (unsigned short)cb;
        }
        u32x4 dd;
        dd[0] = hh ? 0u : (__float_as_uint(d2) >> 16);
        dd[1] = 0; dd[2] = 0; dd[3] = 0;

        f32x16 a0, a1;
#pragma unroll
        for (int i = 0; i < 16; ++i) { a0[i] = 0.f; a1[i] = 0.f; }
#define STEP(K, AV)                                                                                  \
  a0 = __builtin_amdgcn_mfma_f32_32x32x16_bf16(__builtin_bit_cast(bf16x8, AV),                     \
                                               __builtin_bit_cast(bf16x8, wl[(2 * K) * 64 + lane]), \
                                               a0, 0, 0, 0);                                        \
  a1 = __builtin_amdgcn_mfma_f32_32x32x16_bf16(__builtin_bit_cast(bf16x8, AV),                     \
                                               __builtin_bit_cast(bf16x8, wl[(2 * K + 1) * 64 + lane]), \
                                               a1, 0, 0, 0);
        STEP(0, hd0) STEP(1, hd1) STEP(2, hd2) STEP(3, hd3)
        STEP(9, dd)
        STEP(8, g.ea)
        STEP(4, g.hs0) STEP(5, g.hs1) STEP(6, g.hs2) STEP(7, g.hs3)
#undef STEP
        u32x4 m00, m01, m10, m11;
#pragma unroll
        for (int i = 0; i < 4; ++i) {
          m00[i] = pk_bf16(silu_f(a0[2 * i]), silu_f(a0[2 * i + 1]));
          m01[i] = pk_bf16(silu_f(a0[8 + 2 * i]), silu_f(a0[8 + 2 * i + 1]));
          m10[i] = pk_bf16(silu_f(a1[2 * i]), silu_f(a1[2 * i + 1]));
          m11[i] = pk_bf16(silu_f(a1[8 + 2 * i]), silu_f(a1[8 + 2 * i + 1]));
        }
        WAVE_FENCE();
        const char* srow = Sw + r * SROW + hh * 8;
        const u32x2 f00 = *(const u32x2*)(srow), f01 = *(const u32x2*)(srow + 16);
        const u32x2 f10 = *(const u32x2*)(srow + 32), f11 = *(const u32x2*)(srow + 48);
        u32x4 sf0 = {f00[0], f00[1], f01[0], f01[1]};
        u32x4 sf1 = {f10[0], f10[1], f11[0], f11[1]};
        Y0 = __builtin_amdgcn_mfma_f32_32x32x16_bf16(__builtin_bit_cast(bf16x8, sf0), __builtin_bit_cast(bf16x8, m00), Y0, 0, 0, 0);
        Y1 = __builtin_amdgcn_mfma_f32_32x32x16_bf16(__builtin_bit_cast(bf16x8, sf0), __builtin_bit_cast(bf16x8, m10), Y1, 0, 0, 0);
        Y0 = __builtin_amdgcn_mfma_f32_32x32x16_bf16(__builtin_bit_cast(bf16x8, sf1), __builtin_bit_cast(bf16x8, m01), Y0, 0, 0, 0);
        Y1 = __builtin_amdgcn_mfma_f32_32x32x16_bf16(__builtin_bit_cast(bf16x8, sf1), __builtin_bit_cast(bf16x8, m11), Y1, 0, 0, 0);
        WAVE_FENCE();
        if (valid) {
          *s_a = 0;
          *s_b = 0;
        }
      };

      if (nj > 0) {
        Gath gA, gB;
        uint2 rc0 = ld_rec(0), rc1 = ld_rec(1);
        gather(rc0, gA);
        for (int j = 0; j < nj; j += 2) {
          const uint2 rc2 = ld_rec(j + 2);
          gather(rc1, gB);
          compute(rc0, gA, j);
          if (j + 1 >= nj) break;
          const uint2 rc3 = ld_rec(j + 3);
          gather(rc2, gA);
          compute(rc1, gB, j + 1);
          rc0 = rc2;
          rc1 = rc3;
        }
      }
      __syncthreads();
      if (total <= (unsigned)LCAP) break;
    }
    c0 = 0; c1 = 0;
    if (g0 + 256 + t < G) {
      c0 = off[(size_t)(g0 + 256 + t) * NBP + beta];
      c1 = off[(size_t)(g0 + 256 + t) * NBP + beta + 1];
    }
  }

  float* Ypart = (float*)arena;
#pragma unroll
  for (int i = 0; i < 16; ++i) {
    const int row = (i & 3) + 8 * (i >> 2) + 4 * hh;
    Ypart[wv * 2048 + row * 64 + r] = Y0[i];
    Ypart[wv * 2048 + row * 64 + 32 + r] = Y1[i];
  }
  __syncthreads();
  float* Yred = Ypart;
  {
    f32x4 a = *(const f32x4*)&Ypart[t * 8], b = *(const f32x4*)&Ypart[t * 8 + 4];
#pragma unroll
    for (int w = 1; w < 4; ++w) {
      a += *(const f32x4*)&Ypart[w * 2048 + t * 8];
      b += *(const f32x4*)&Ypart[w * 2048 + t * 8 + 4];
    }
    *(f32x4*)&Yred[t * 8] = a;
    *(f32x4*)&Yred[t * 8 + 4] = b;
  }
  __syncthreads();
  {
    const int row = t >> 5, c2 = (t & 31) * 2;
    *(unsigned*)&cat[row][64 + c2] = pk_bf16(Yred[row * 64 + c2], Yred[row * 64 + c2 + 1]);
  }
  if (t < 192) {
    const int pr = t >> 3, part = t & 7;
    const float* yr = &Yred[(8 + pr) * 64 + part * 8];
    float sacc = 0.f;
#pragma unroll
    for (int c = 0; c < 8; ++c) sacc += yr[c] * wxs[part * 8 + c];
    sacc += __shfl_xor(sacc, 1);
    sacc += __shfl_xor(sacc, 2);
    sacc += __shfl_xor(sacc, 4);
    const int node = pr & 7, d = pr >> 3;
    if (part == 0 && n0 + node < N) {
      const float v = posd[node][d] + rbf(sacc);
      out_pos[(size_t)(n0 + node) * 3 + d] = (unsigned short)(pk_bf16(v, v) & 0xffff);
    }
  }
  __syncthreads();
  {
    f32x4 acc = {0.f, 0.f, 0.f, 0.f};
    const int arow = lane & 7, kq = lane >> 4;
#pragma unroll
    for (int s = 0; s < 4; ++s) {
      const u32x4 af = *(const u32x4*)&cat[arow][32 * s + 8 * kq];
      const u32x4 bfr = whl[(wv * 4 + s) * 64 + lane];
      acc = __builtin_amdgcn_mfma_f32_16x16x32_bf16(__builtin_bit_cast(bf16x8, af), __builtin_bit_cast(bf16x8, bfr), acc, 0, 0, 0);
    }
    if (lane < 32) {
      const int col = 16 * wv + (lane & 15);
#pragma unroll
      for (int i = 0; i < 4; ++i) {
        const int row = (lane >> 4) * 4 + i;
        if (n0 + row < N) {
          const float u = rbf(silu_f(rbf(acc[i])));
          const float v = bf2f(cat[row][col]) + u;
          out_h[(size_t)(n0 + row) * D + col] = (unsigned short)(pk_bf16(v, v) & 0xffff);
        }
      }
    }
  }
  __syncthreads();
  }
}

extern "C" void kernel_launch(void* const* d_in, const int* in_sizes, int n_in, void* d_out,
                              int out_size, void* d_ws, size_t ws_size, hipStream_t stream) {
  const unsigned short* h = (const unsigned short*)d_in[0];
  const unsigned short* pos = (const unsigned short*)d_in[1];
  const unsigned short* e_attr = (const unsigned short*)d_in[2];
  const int* src = (const int*)d_in[3];
  const int* dst = (const int*)d_in[4];
  const unsigned short* w_e = (const unsigned short*)d_in[5];
  const unsigned short* w_x = (const unsigned short*)d_in[6];
  const unsigned short* w_h = (const unsigned short*)d_in[7];
  const int N = in_sizes[0] / D;
  const int E = in_sizes[3];
  const int NB = (N + 7) / 8;
  const int G = (E + CH - 1) / CH;
  const int NBP = (NB + 2) & ~1;

  char* ws = (char*)d_ws;
  size_t o = 0;
  uint2* recs = (uint2*)(ws + o); o += (size_t)E * 8; o = (o + 255) & ~(size_t)255;
  unsigned short* off = (unsigned short*)(ws + o); o += (size_t)G * NBP * 2; o = (o + 255) & ~(size_t)255;
  u32x4* wpack = (u32x4*)(ws + o); o += 20 * 64 * 16;
  u32x4* whpack = (u32x4*)(ws + o); o += 16 * 64 * 16;
  uint2* posP = (uint2*)(ws + o); o += (size_t)N * 8;

  unsigned short* out_h = (unsigned short*)d_out;
  unsigned short* out_pos = out_h + (size_t)N * D;

  sort_kernel<<<G + 2, SORT_T, 0, stream>>>(src, dst, E, NB, G, recs, off, NBP, w_e, w_h, pos, N,
                                           wpack, whpack, posP);
  const int per = (NB + 7) / 8;
  const int nwx = per < 64 ? per : 64;
  main_kernel<<<nwx * 8, 256, 0, stream>>>(h, e_attr, w_x, recs, off, NBP, G, NB, N, wpack, whpack,
                                           posP, out_h, out_pos);
}
```

```cpp
#include <hip/hip_runtime.h>
#include <hip/hip_bf16.h>
#include <stdint.h>

typedef __attribute__((ext_vector_type(8))) short bf16x8;
typedef __attribute__((ext_vector_type(16))) float f32x16;
typedef __attribute__((ext_vector_type(4))) float f32x4;
typedef __attribute__((ext_vector_type(2))) float f32x2;
typedef __attribute__((ext_vector_type(2))) __bf16 bfx2;
typedef __attribute__((ext_vector_type(4))) unsigned u32x4;
typedef __attribute__((ext_vector_type(2))) unsigned u32x2;

#define DI __device__ __forceinline__
#define WAVE_FENCE()                                         \
  do {                                                       \
    __builtin_amdgcn_fence(__ATOMIC_SEQ_CST, "wavefront");   \
    __builtin_amdgcn_wave_barrier();                         \
  } while (0)

static constexpr int D = 64;
static constexpr int DE = 16;
static constexpr int CH = 5120;
static constexpr int SORT_T = 1024;
static constexpr int MAXNB = 2048;
static constexpr int LCAP = 1024;
static constexpr int SROW = 72;

DI float bf2f(unsigned short u) { return __uint_as_float(((unsigned)u) << 16); }
DI unsigned pk_bf16(float a, float b) {
  f32x2 v = {a, b};
  bfx2 r = __builtin_convertvector(v, bfx2);
  return __builtin_bit_cast(unsigned, r);
}
DI float rbf(float a) { return __uint_as_float(pk_bf16(a, a) << 16); }
DI float silu_f(float x) {
  float t = __builtin_amdgcn_exp2f(x * -1.44269504088896340736f);
  return x * __builtin_amdgcn_rcpf(1.0f + t);
}

__global__ __launch_bounds__(SORT_T) void sort_kernel(
    const int* __restrict__ src, const int* __restrict__ dst, int E, int NB, int G,
    uint2* __restrict__ recs, unsigned short* __restrict__ off, int NBP,
    const unsigned short* __restrict__ w_e, const unsigned short* __restrict__ w_h,
    const unsigned short* __restrict__ pos, int N,
    u32x4* __restrict__ wpack, u32x4* __restrict__ whpack, uint2* __restrict__ posP) {
  __shared__ unsigned hist[MAXNB + 32];
  __shared__ unsigned wsum[16];
  __shared__ uint2 stage[CH];
  const int t = threadIdx.x;
  const int b = blockIdx.x;
  if (b >= G) {
    const int pb = b - G;
    if (pb == 0) {
      for (int i = t; i < 20 * 64; i += SORT_T) {
        const int f = i >> 6, l = i & 63;
        const int kap = f >> 1, nu = f & 1;
        const int col = 32 * nu + (l & 31);
        unsigned short v[8];
#pragma unroll
        for (int j = 0; j < 8; ++j) {
          const int kk = 8 * (l >> 5) + j;
          int row;
          if (kap < 8) row = 16 * kap + kk;
          else if (kap == 8) row = 129 + kk;
          else row = (kk == 0) ? 128 : -1;
          v[j] = (row >= 0) ? w_e[row * D + col] : (unsigned short)0;
        }
        u32x4 p;
        p[0] = v[0] | ((unsigned)v[1] << 16);
        p[1] = v[2] | ((unsigned)v[3] << 16);
        p[2] = v[4] | ((unsigned)v[5] << 16);
        p[3] = v[6] | ((unsigned)v[7] << 16);
        wpack[i] = p;
      }
      for (int i = t; i < 16 * 64; i += SORT_T) {
        const int f = i >> 6, l = i & 63;
        const int w = f >> 2, s = f & 3;
        const int col = 16 * w + (l & 15);
        unsigned short v[8];
#pragma unroll
        for (int j = 0; j < 8; ++j) v[j] = w_h[(32 * s + 8 * (l >> 4) + j) * D + col];
        u32x4 p;
        p[0] = v[0] | ((unsigned)v[1] << 16);
        p[1] = v[2] | ((unsigned)v[3] << 16);
        p[2] = v[4] | ((unsigned)v[5] << 16);
        p[3] = v[6] | ((unsigned)v[7] << 16);
        whpack[i] = p;
      }
    } else {
      for (int n = t; n < N; n += SORT_T) {
        uint2 p;
        p.x = pos[n * 3] | ((unsigned)pos[n * 3 + 1] << 16);
        p.y = pos[n * 3 + 2];
        posP[n] = p;
      }
    }
    return;
  }
  const int e0 = b * CH;
  const int len = min(CH, E - e0);
  for (int i = t; i < NB + 1; i += SORT_T) hist[i] = 0;
  __syncthreads();
  int dv[CH / SORT_T], sv[CH / SORT_T];
#pragma unroll
  for (int k = 0; k < CH / SORT_T; ++k) {
    const int i = t + k * SORT_T;
    if (i < len) {
      dv[k] = dst[e0 + i];
      sv[k] = src[e0 + i];
    } else {
      dv[k] = -1;
      sv[k] = 0;
    }
  }
#pragma unroll
  for (int k = 0; k < CH / SORT_T; ++k)
    if (dv[k] >= 0) atomicAdd(&hist[dv[k] >> 3], 1u);
  __syncthreads();
  {
    const int i0 = 2 * t;
    const unsigned a = (i0 < NB) ? hist[i0] : 0u;
    const unsigned c = (i0 + 1 < NB) ? hist[i0 + 1] : 0u;
    unsigned s = a + c;
    const int lane = t & 63, wv = t >> 6;
    unsigned incl = s;
#pragma unroll
    for (int d = 1; d < 64; d <<= 1) {
      unsigned o = __shfl_up(incl, d);
      if (lane >= d) incl += o;
    }
    if (lane == 63) wsum[wv] = incl;
    __syncthreads();
    unsigned base = 0;
    for (int w = 0; w < wv; ++w) base += wsum[w];
    const unsigned ex = base + incl - s;
    if (i0 < NB) hist[i0] = ex;
    if (i0 + 1 < NB) hist[i0 + 1] = ex + a;
    if (t == 0) hist[NB] = (unsigned)len;
  }
  __syncthreads();
  for (int i = t; i < NB + 1; i += SORT_T) off[(size_t)b * NBP + i] = (unsigned short)hist[i];
  __syncthreads();
#pragma unroll
  for (int k = 0; k < CH / SORT_T; ++k) {
    if (dv[k] >= 0) {
      const unsigned p = atomicAdd(&hist[dv[k] >> 3], 1u);
      uint2 r;
      r.x = (unsigned)(e0 + t + k * SORT_T);
      r.y = (unsigned)sv[k] | ((unsigned)(dv[k] & 7) << 28);
      stage[p] = r;
    }
  }
  __syncthreads();
  for (int i = t; i < len; i += SORT_T) recs[e0 + i] = stage[i];
}

__global__ __launch_bounds__(256, 2) void main_kernel(
    const unsigned short* __restrict__ h, const unsigned short* __restrict__ e_attr,
    const unsigned short* __restrict__ w_x, const uint2* __restrict__ recs,
    const unsigned short* __restrict__ off, int NBP, int G, int NB, int N,
    const u32x4* __restrict__ wpack, const u32x4* __restrict__ whpack,
    const uint2* __restrict__ posP, unsigned short* __restrict__ out_h,
    unsigned short* __restrict__ out_pos) {
  __shared__ u32x4 wl[8 * 64];
  __shared__ u32x4 whl[16 * 64];
  __shared__ __attribute__((aligned(16))) char arena[32 * 1024];
  unsigned* ridx = (unsigned*)arena;
  char(*Sm)[32 * SROW] = (char(*)[32 * SROW])(arena + LCAP * 4);
  __shared__ __attribute__((aligned(16))) unsigned short cat[8][136];
  __shared__ __attribute__((aligned(16))) float posd[8][4];
  __shared__ float wxs[64];
  __shared__ unsigned wtot[4];

  const int t = threadIdx.x, lane = t & 63, wv = t >> 6;
  const int r = lane & 31, hh = lane >> 5;
  const int per = (NB + 7) >> 3;
  const int nwx = (int)(gridDim.x >> 3);
  const int xcd = blockIdx.x & 7;
  int bl = blockIdx.x >> 3;
  if (bl >= per || xcd * per + bl >= NB) return;

  unsigned o0 = 0, o1 = 0;
  u32x4 hrow = {0u, 0u, 0u, 0u};
  uint2 prow = {0u, 0u};
  auto prefetch_bucket = [&](int beta_) {
    if (t < G) {
      o0 = off[(size_t)t * NBP + beta_];
      o1 = off[(size_t)t * NBP + beta_ + 1];
    }
    if (t < 64) {
      const int n = min(beta_ * 8 + (t >> 3), N - 1);
      hrow = *(const u32x4*)&h[(size_t)n * D + (t & 7) * 8];
    } else if (t < 72) {
      prow = posP[min(beta_ * 8 + t - 64, N - 1)];
    }
  };
  prefetch_bucket(xcd * per + bl);
#pragma unroll
  for (int i = 0; i < 2; ++i) wl[t + 256 * i] = wpack[t + 256 * i];
  u32x4 bw[12];
#pragma unroll
  for (int i = 0; i < 10; ++i) bw[i] = wpack[(8 + i) * 64 + lane];
#pragma unroll
  for (int i = 10; i < 12; ++i) {
    bw[i][0] = ((const unsigned*)wpack)[((8 + i) * 64 + lane) * 4];
    bw[i][1] = 0u; bw[i][2] = 0u; bw[i][3] = 0u;
  }
#pragma unroll
  for (int i = 0; i < 4; ++i) whl[t + 256 * i] = whpack[t + 256 * i];
  if (t < 64) wxs[t] = bf2f(w_x[t]);
  char* Sw = Sm[wv];

  for (; bl < per && xcd * per + bl < NB; bl += nwx) {
  const int beta = xcd * per + bl;
  const int n0 = beta * 8;
  if (t < 64) {
    *(u32x4*)&cat[t >> 3][(t & 7) * 8] = hrow;
  } else if (t < 72) {
    const int row = t - 64;
    posd[row][0] = bf2f(prow.x & 0xffff);
    posd[row][1] = bf2f(prow.x >> 16);
    posd[row][2] = bf2f(prow.y & 0xffff);
    posd[row][3] = 0.f;
  }
  unsigned c0 = o0, c1 = o1;
  if (bl + nwx < per && beta + nwx < NB) prefetch_bucket(beta + nwx);
  for (int i = t; i < 4 * 32 * SROW / 4; i += 256) ((unsigned*)Sm)[i] = 0;

  f32x16 Y0, Y1;
#pragma unroll
  for (int i = 0; i < 16; ++i) { Y0[i] = 0.f; Y1[i] = 0.f; }

  struct Gath { u32x4 hs0, hs1, hs2, hs3, ea; uint2 ps; };

  for (int g0 = 0; g0 < G; g0 += 256) {
    unsigned rem = c1 - c0;
    unsigned rptr = (unsigned)(g0 + t) * CH + c0;
    while (true) {
      unsigned incl = rem;
#pragma unroll
      for (int d = 1; d < 64; d <<= 1) {
        unsigned o = __shfl_up(incl, d);
        if (lane >= d) incl += o;
      }
      if (lane == 63) wtot[wv] = incl;
      __syncthreads();
      unsigned base = 0, total = 0;
#pragma unroll
      for (int w = 0; w < 4; ++w) {
        const unsigned x = wtot[w];
        if (w < wv) base += x;
        total += x;
      }
      if (total == 0) { __syncthreads(); break; }
      const unsigned pre = base + incl - rem;
      unsigned take = 0;
      if (pre < (unsigned)LCAP) take = min(rem, (unsigned)LCAP - pre);
      for (unsigned i = 0; i < take; ++i) ridx[pre + i] = rptr + i;
      rem -= take;
      rptr += take;
      const int cnt = (int)min(total, (unsigned)LCAP);
      __syncthreads();

      u32x4 bA0, bA1;
      {
        f32x16 c0v, c1v;
#pragma unroll
        for (int i = 0; i < 16; ++i) { c0v[i] = 0.f; c1v[i] = 0.f; }
        const u32x4* hd_p = (const u32x4*)(&cat[r & 7][hh * 8]);
#pragma unroll
        for (int k = 0; k < 4; ++k) {
          const u32x4 hd = hd_p[2 * k];
          c0v = __builtin_amdgcn_mfma_f32_32x32x16_bf16(__builtin_bit_cast(bf16x8, hd), __builtin_bit_cast(bf16x8, wl[(2 * k) * 64 + lane]), c0v, 0, 0, 0);
          c1v = __builtin_amdgcn_mfma_f32_32x32x16_bf16(__builtin_bit_cast(bf16x8, hd), __builtin_bit_cast(bf16x8, wl[(2 * k + 1) * 64 + lane]), c1v, 0, 0, 0);
        }
        bA0[0] = pk_bf16(c0v[0], c0v[1]);
        bA0[1] = pk_bf16(c0v[2], c0v[3]);
        bA0[2] = pk_bf16(c0v[0] - __uint_as_float(bA0[0] << 16), c0v[1] - __uint_as_float(bA0[0] & 0xffff0000u));
        bA0[3] = pk_bf16(c0v[2] - __uint_as_float(bA0[1] << 16), c0v[3] - __uint_as_float(bA0[1] & 0xffff0000u));
        bA1[0] = pk_bf16(c1v[0], c1v[1]);
        bA1[1] = pk_bf16(c1v[2], c1v[3]);
        bA1[2] = pk_bf16(c1v[0] - __uint_as_float(bA1[0] << 16), c1v[1] - __uint_as_float(bA1[0] & 0xffff0000u));
        bA1[3] = pk_bf16(c1v[2] - __uint_as_float(bA1[1] << 16), c1v[3] - __uint_as_float(bA1[1] & 0xffff0000u));
      }
      const int ntiles = (cnt + 31) >> 5;
      const int nj = (ntiles - wv + 3) >> 2;
      auto ld_rec = [&](int j) -> uint2 {
        const int idx = min((wv + 4 * j) * 32 + r, cnt - 1);
        return recs[ridx[idx]];
      };
      auto gather = [&](const uint2 rc, Gath& g) {
        const unsigned e = rc.x, s = rc.y & 0x0FFFFFFFu;
        const u32x4* hs_p = (const u32x4*)(h + (size_t)s * D + hh * 8);
        g.hs0 = hs_p[0]; g.hs1 = hs_p[2]; g.hs2 = hs_p[4]; g.hs3 = hs_p[6];
        g.ea = *(const u32x4*)(e_attr + (size_t)e * DE + hh * 8);
        g.ps = posP[s];
      };
      auto compute = [&](const uint2 rc, Gath& g, int j, const uint2 rcn) {
        const bool valid = (wv + 4 * j) * 32 + r < cnt;
        const unsigned dl = rc.y >> 28;
        const unsigned ohv = (dl & 1) ? 0x3f800000u : 0x3f80u;
        u32x4 oh;
        oh[0] = ((dl >> 1) == (unsigned)(2 * hh)) ? ohv : 0u;
        oh[1] = ((dl >> 1) == (unsigned)(2 * hh + 1)) ? ohv : 0u;
        oh[2] = oh[0];
        oh[3] = oh[1];
        const f32x4 pd = *(const f32x4*)&posd[dl][0];
        const float dx = rbf(pd[0] - bf2f(g.ps.x & 0xffff));
        const float dy = rbf(pd[1] - bf2f(g.ps.x >> 16));
        const float dz = rbf(pd[2] - bf2f(g.ps.y & 0xffff));
        const float d2 = rbf(rbf(dx * dx) + rbf(dy * dy) + rbf(dz * dz));
        unsigned short* s_a = (unsigned short*)(Sw + (hh * 16 + dl) * SROW) + r;
        unsigned short* s_b = (unsigned short*)(Sw + (hh * 16 + 8 + dl) * SROW) + r;
        if (valid) {
          const unsigned ca = hh ? (__float_as_uint(dy) >> 16) : 0x3f80u;
          const unsigned cb = hh ? (__float_as_uint(dz) >> 16) : (__float_as_uint(dx) >> 16);
          *s_a = (unsigned short)ca;
          *s_b = (unsigned short)cb;
        }
        u32x4 dd;
        dd[0] = hh ? 0u : (__float_as_uint(d2) >> 16);
        dd[1] = 0; dd[2] = 0; dd[3] = 0;

        f32x16 a0, a1;
#pragma unroll
        for (int i = 0; i < 16; ++i) { a0[i] = 0.f; a1[i] = 0.f; }
#define STEP(AV, B0, B1)                                                                                     \
  a0 = __builtin_amdgcn_mfma_f32_32x32x16_bf16(__builtin_bit_cast(bf16x8, AV), __builtin_bit_cast(bf16x8, B0), a0, 0, 0, 0); \
  a1 = __builtin_amdgcn_mfma_f32_32x32x16_bf16(__builtin_bit_cast(bf16x8, AV), __builtin_bit_cast(bf16x8, B1), a1, 0, 0, 0);
        STEP(oh, bA0, bA1)
        STEP(dd, bw[10], bw[11])
        STEP(g.ea, bw[8], bw[9])
        STEP(g.hs0, bw[0], bw[1]) STEP(g.hs1, bw[2], bw[3]) STEP(g.hs2, bw[4], bw[5]) STEP(g.hs3, bw[6], bw[7])
#undef STEP
        gather(rcn, g);
        u32x4 m00, m01, m10, m11;
#pragma unroll
        for (int i = 0; i < 4; ++i) {
          m00[i] = pk_bf16(silu_f(a0[2 * i]), silu_f(a0[2 * i + 1]));
          m01[i] = pk_bf16(silu_f(a0[8 + 2 * i]), silu_f(a0[8 + 2 * i + 1]));
          m10[i] = pk_bf16(silu_f(a1[2 * i]), silu_f(a1[2 * i + 1]));
          m11[i] = pk_bf16(silu_f(a1[8 + 2 * i]), silu_f(a1[8 + 2 * i + 1]));
        }
        WAVE_FENCE();
        const char* srow = Sw + r * SROW + hh * 8;
        const u32x2 f00 = *(const u32x2*)(srow), f01 = *(const u32x2*)(srow + 16);
        const u32x2 f10 = *(const u32x2*)(srow + 32), f11 = *(const u32x2*)(srow + 48);
        u32x4 sf0 = {f00[0], f00[1], f01[0], f01[1]};
        u32x4 sf1 = {f10[0], f10[1], f11[0], f11[1]};
        Y0 = __builtin_amdgcn_mfma_f32_32x32x16_bf16(__builtin_bit_cast(bf16x8, sf0), __builtin_bit_cast(bf16x8, m00), Y0, 0, 0, 0);
        Y1 = __builtin_amdgcn_mfma_f32_32x32x16_bf16(__builtin_bit_cast(bf16x8, sf0), __builtin_bit_cast(bf16x8, m10), Y1, 0, 0, 0);
        Y0 = __builtin_amdgcn_mfma_f32_32x32x16_bf16(__builtin_bit_cast(bf16x8, sf1), __builtin_bit_cast(bf16x8, m01), Y0, 0, 0, 0);
        Y1 = __builtin_amdgcn_mfma_f32_32x32x16_bf16(__builtin_bit_cast(bf16x8, sf1), __builtin_bit_cast(bf16x8, m11), Y1, 0, 0, 0);
        WAVE_FENCE();
        if (valid) {
          *s_a = 0;
          *s_b = 0;
        }
      };

      if (nj > 0) {
        Gath g;
        uint2 rc0 = ld_rec(0), rc1 = ld_rec(1);
        gather(rc0, g);
        for (int j = 0; j < nj; ++j) {
          const uint2 rc2 = ld_rec(j + 2);
          compute(rc0, g, j, rc1);
          rc0 = rc1;
          rc1 = rc2;
        }
      }
      __syncthreads();
      if (total <= (unsigned)LCAP) break;
    }
    c0 = 0; c1 = 0;
    if (g0 + 256 + t < G) {
      c0 = off[(size_t)(g0 + 256 + t) * NBP + beta];
      c1 = off[(size_t)(g0 + 256 + t) * NBP + beta + 1];
    }
  }

  float* Ypart = (float*)arena;
#pragma unroll
  for (int i = 0; i < 16; ++i) {
    const int row = (i & 3) + 8 * (i >> 2) + 4 * hh;
    Ypart[wv * 2048 + row * 64 + r] = Y0[i];
    Ypart[wv * 2048 + row * 64 + 32 + r] = Y1[i];
  }
  __syncthreads();
  float* Yred = Ypart;
  {
    f32x4 a = *(const f32x4*)&Ypart[t * 8], b = *(const f32x4*)&Ypart[t * 8 + 4];
#pragma unroll
    for (int w = 1; w < 4; ++w) {
      a += *(const f32x4*)&Ypart[w * 2048 + t * 8];
      b += *(const f32x4*)&Ypart[w * 2048 + t * 8 + 4];
    }
    *(f32x4*)&Yred[t * 8] = a;
    *(f32x4*)&Yred[t * 8 + 4] = b;
  }
  __syncthreads();
  {
    const int row = t >> 5, c2 = (t & 31) * 2;
    *(unsigned*)&cat[row][64 + c2] = pk_bf16(Yred[row * 64 + c2], Yred[row * 64 + c2 + 1]);
  }
  if (t < 192) {
    const int pr = t >> 3, part = t & 7;
    const float* yr = &Yred[(8 + pr) * 64 + part * 8];
    float sacc = 0.f;
#pragma unroll
    for (int c = 0; c < 8; ++c) sacc += yr[c] * wxs[part * 8 + c];
    sacc += __shfl_xor(sacc, 1);
    sacc += __shfl_xor(sacc, 2);
    sacc += __shfl_xor(sacc, 4);
    const int node = pr & 7, d = pr >> 3;
    if (part == 0 && n0 + node < N) {
      const float v = posd[node][d] + rbf(sacc);
      out_pos[(size_t)(n0 + node) * 3 + d] = (unsigned short)(pk_bf16(v, v) & 0xffff);
    }
  }
  __syncthreads();
  {
    f32x4 acc = {0.f, 0.f, 0.f, 0.f};
    const int arow = lane & 7, kq = lane >> 4;
#pragma unroll
    for (int s = 0; s < 4; ++s) {
      const u32x4 af = *(const u32x4*)&cat[arow][32 * s + 8 * kq];
      const u32x4 bfr = whl[(wv * 4 + s) * 64 + lane];
      acc = __builtin_amdgcn_mfma_f32_16x16x32_bf16(__builtin_bit_cast(bf16x8, af), __builtin_bit_cast(bf16x8, bfr), acc, 0, 0, 0);
    }
    if (lane < 32) {
      const int col = 16 * wv + (lane & 15);
#pragma unroll
      for (int i = 0; i < 4; ++i) {
        const int row = (lane >> 4) * 4 + i;
        if (n0 + row < N) {
          const float u = rbf(silu_f(rbf(acc[i])));
          const float v = bf2f(cat[row][col]) + u;
          out_h[(size_t)(n0 + row) * D + col] = (unsigned short)(pk_bf16(v, v) & 0xffff);
        }
      }
    }
  }
  __syncthreads();
  }
}

extern "C" void kernel_launch(void* const* d_in, const int* in_sizes, int n_in, void* d_out,
                              int out_size, void* d_ws, size_t ws_size, hipStream_t stream) {
  const unsigned short* h = (const unsigned short*)d_in[0];
  const unsigned short* pos = (const unsigned short*)d_in[1];
  const unsigned short* e_attr = (const unsigned short*)d_in[2];
  const int* src = (const int*)d_in[3];
  const int* dst = (const int*)d_in[4];
  const unsigned short* w_e = (const unsigned short*)d_in[5];
  const unsigned short* w_x = (const unsigned short*)d_in[6];
  const unsigned short* w_h = (const unsigned short*)d_in[7];
  const int N = in_sizes[0] / D;
  const int E = in_sizes[3];
  const int NB = (N + 7) / 8;
  const int G = (E + CH - 1) / CH;
  const int NBP = (NB + 2) & ~1;

  char* ws = (char*)d_ws;
  size_t o = 0;
  uint2* recs = (uint2*)(ws + o); o += (size_t)E * 8; o = (o + 255) & ~(size_t)255;
  unsigned short* off = (unsigned short*)(ws + o); o += (size_t)G * NBP * 2; o = (o + 255) & ~(size_t)255;
  u32x4* wpack = (u32x4*)(ws + o); o += 20 * 64 * 16;
  u32x4* whpack = (u32x4*)(ws + o); o += 16 * 64 * 16;
  uint2* posP = (uint2*)(ws + o); o += (size_t)N * 8;

  unsigned short* out_h = (unsigned short*)d_out;
  unsigned short* out_pos = out_h + (size_t)N * D;

  sort_kernel<<<G + 2, SORT_T, 0, stream>>>(src, dst, E, NB, G, recs, off, NBP, w_e, w_h, pos, N,
                                           wpack, whpack, posP);
  const int per = (NB + 7) / 8;
  const int nwx = per < 64 ? per : 64;
  main_kernel<<<nwx * 8, 256, 0, stream>>>(h, e_attr, w_x, recs, off, NBP, G, NB, N, wpack, whpack,
                                           posP, out_h, out_pos);
}
```

```cpp
#include <hip/hip_runtime.h>
#include <hip/hip_bf16.h>
#include <stdint.h>

typedef __attribute__((ext_vector_type(8))) short bf16x8;
typedef __attribute__((ext_vector_type(16))) float f32x16;
typedef __attribute__((ext_vector_type(4))) float f32x4;
typedef __attribute__((ext_vector_type(2))) float f32x2;
typedef __attribute__((ext_vector_type(2))) __bf16 bfx2;
typedef __attribute__((ext_vector_type(4))) unsigned u32x4;
typedef __attribute__((ext_vector_type(2))) unsigned u32x2;

#define DI __device__ __forceinline__
#define WAVE_FENCE()                                         \
  do {                                                       \
    __builtin_amdgcn_fence(__ATOMIC_SEQ_CST, "wavefront");   \
    __builtin_amdgcn_wave_barrier();                         \
  } while (0)

static constexpr int D = 64;
static constexpr int DE = 16;
static constexpr int CH = 5120;
static constexpr int SORT_T = 1024;
static constexpr int MAXNB = 2048;
static constexpr int LCAP = 1024;
static constexpr int SROW = 72;

DI float bf2f(unsigned short u) { return __uint_as_float(((unsigned)u) << 16); }
DI unsigned pk_bf16(float a, float b) {
  f32x2 v = {a, b};
  bfx2 r = __builtin_convertvector(v, bfx2);
  return __builtin_bit_cast(unsigned, r);
}
DI float rbf(float a) { return __uint_as_float(pk_bf16(a, a) << 16); }
DI float silu_f(float x) {
  float t = __builtin_amdgcn_exp2f(x * -1.44269504088896340736f);
  return x * __builtin_amdgcn_rcpf(1.0f + t);
}

__global__ __launch_bounds__(SORT_T) void sort_kernel(
    const int* __restrict__ src, const int* __restrict__ dst, int E, int NB, int G,
    uint2* __restrict__ recs, unsigned short* __restrict__ off, int NBP,
    const unsigned short* __restrict__ w_e, const unsigned short* __restrict__ w_h,
    const unsigned short* __restrict__ pos, int N,
    u32x4* __restrict__ wpack, u32x4* __restrict__ whpack, uint2* __restrict__ posP) {
  __shared__ unsigned hist[MAXNB + 32];
  __shared__ unsigned wsum[16];
  __shared__ uint2 stage[CH];
  const int t = threadIdx.x;
  const int b = blockIdx.x;
  if (b >= G) {
    const int pb = b - G;
    if (pb == 0) {
      for (int i = t; i < 20 * 64; i += SORT_T) {
        const int f = i >> 6, l = i & 63;
        const int kap = f >> 1, nu = f & 1;
        const int col = 32 * nu + (l & 31);
        unsigned short v[8];
#pragma unroll
        for (int j = 0; j < 8; ++j) {
          const int kk = 8 * (l >> 5) + j;
          int row;
          if (kap < 8) row = 16 * kap + kk;
          else if (kap == 8) row = 129 + kk;
          else row = (kk == 0) ? 128 : -1;
          v[j] = (row >= 0) ? w_e[row * D + col] : (unsigned short)0;
        }
        u32x4 p;
        p[0] = v[0] | ((unsigned)v[1] << 16);
        p[1] = v[2] | ((unsigned)v[3] << 16);
        p[2] = v[4] | ((unsigned)v[5] << 16);
        p[3] = v[6] | ((unsigned)v[7] << 16);
        wpack[i] = p;
      }
      for (int i = t; i < 16 * 64; i += SORT_T) {
        const int f = i >> 6, l = i & 63;
        const int w = f >> 2, s = f & 3;
        const int col = 16 * w + (l & 15);
        unsigned short v[8];
#pragma unroll
        for (int j = 0; j < 8; ++j) v[j] = w_h[(32 * s + 8 * (l >> 4) + j) * D + col];
        u32x4 p;
        p[0] = v[0] | ((unsigned)v[1] << 16);
        p[1] = v[2] | ((unsigned)v[3] << 16);
        p[2] = v[4] | ((unsigned)v[5] << 16);
        p[3] = v[6] | ((unsigned)v[7] << 16);
        whpack[i] = p;
      }
    } else {
      for (int n = t; n < N; n += SORT_T) {
        uint2 p;
        p.x = pos[n * 3] | ((unsigned)pos[n * 3 + 1] << 16);
        p.y = pos[n * 3 + 2];
        posP[n] = p;
      }
    }
    return;
  }
  const int e0 = b * CH;
  const int len = min(CH, E - e0);
  for (int i = t; i < NB + 1; i += SORT_T) hist[i] = 0;
  __syncthreads();
  int dv[CH / SORT_T], sv[CH / SORT_T];
#pragma unroll
  for (int k = 0; k < CH / SORT_T; ++k) {
    const int i = t + k * SORT_T;
    if (i < len) {
      dv[k] = dst[e0 + i];
      sv[k] = src[e0 + i];
    } else {
      dv[k] = -1;
      sv[k] = 0;
    }
  }
#pragma unroll
  for (int k = 0; k < CH / SORT_T; ++k)
    if (dv[k] >= 0) atomicAdd(&hist[dv[k] >> 3], 1u);
  __syncthreads();
  {
    const int i0 = 2 * t;
    const unsigned a = (i0 < NB) ? hist[i0] : 0u;
    const unsigned c = (i0 + 1 < NB) ? hist[i0 + 1] : 0u;
    unsigned s = a + c;
    const int lane = t & 63, wv = t >> 6;
    unsigned incl = s;
#pragma unroll
    for (int d = 1; d < 64; d <<= 1) {
      unsigned o = __shfl_up(incl, d);
      if (lane >= d) incl += o;
    }
    if (lane == 63) wsum[wv] = incl;
    __syncthreads();
    unsigned base = 0;
    for (int w = 0; w < wv; ++w) base += wsum[w];
    const unsigned ex = base + incl - s;
    if (i0 < NB) hist[i0] = ex;
    if (i0 + 1 < NB) hist[i0 + 1] = ex + a;
    if (t == 0) hist[NB] = (unsigned)len;
  }
  __syncthreads();
  for (int i = t; i < NB + 1; i += SORT_T) off[(size_t)b * NBP + i] = (unsigned short)hist[i];
  __syncthreads();
#pragma unroll
  for (int k = 0; k < CH / SORT_T; ++k) {
    if (dv[k] >= 0) {
      const unsigned p = atomicAdd(&hist[dv[k] >> 3], 1u);
      uint2 r;
      r.x = (unsigned)(e0 + t + k * SORT_T);
      r.y = (unsigned)sv[k] | ((unsigned)(dv[k] & 7) << 28);
      stage[p] = r;
    }
  }
  __syncthreads();
  for (int i = t; i < len; i += SORT_T) recs[e0 + i] = stage[i];
}

__global__ __launch_bounds__(256, 2) void main_kernel(
    const unsigned short* __restrict__ h, const unsigned short* __restrict__ e_attr,
    const unsigned short* __restrict__ w_x, const uint2* __restrict__ recs,
    const unsigned short* __restrict__ off, int NBP, int G, int NB, int N,
    const u32x4* __restrict__ wpack, const u32x4* __restrict__ whpack,
    const uint2* __restrict__ posP, unsigned short* __restrict__ out_h,
    unsigned short* __restrict__ out_pos) {
  __shared__ u32x4 wl[10 * 64];
  __shared__ u32x4 whl[16 * 64];
  __shared__ __attribute__((aligned(16))) float Ypart[4 * 2048];
  __shared__ unsigned ridx[2][LCAP];
  __shared__ __attribute__((aligned(16))) char Sm[4][32 * SROW];
  __shared__ __attribute__((aligned(16))) unsigned short cat[2][8][136];
  __shared__ __attribute__((aligned(16))) float posd[2][8][4];
  __shared__ float wxs[64];
  __shared__ unsigned wtot[4];

  const int t = threadIdx.x, lane = t & 63, wv = t >> 6;
  const int r = lane & 31, hh = lane >> 5;
  const int per = (NB + 7) >> 3;
  const int nwx = (int)(gridDim.x >> 3);
  const int xcd = blockIdx.x & 7;
  int bl = blockIdx.x >> 3;
  if (bl >= per || xcd * per + bl >= NB) return;

  unsigned o0 = 0, o1 = 0;
  u32x4 hrow = {0u, 0u, 0u, 0u};
  auto prefetch_bucket = [&](int beta_) {
    if (t < G) {
      o0 = off[(size_t)t * NBP + beta_];
      o1 = off[(size_t)t * NBP + beta_ + 1];
    }
    if (t < 64) {
      const int n = min(beta_ * 8 + (t >> 3), N - 1);
      hrow = *(const u32x4*)&h[(size_t)n * D + (t & 7) * 8];
    } else if (t < 72) {
      const uint2 p = posP[min(beta_ * 8 + t - 64, N - 1)];
      hrow[0] = p.x;
      hrow[1] = p.y;
    }
  };
  prefetch_bucket(xcd * per + bl);
#pragma unroll
  for (int i = 0; i < 2; ++i) wl[t + 256 * i] = wpack[t + 256 * i];
  if (t < 128) wl[512 + t] = wpack[16 * 64 + t];
  u32x4 bw[12];
#pragma unroll
  for (int i = 0; i < 8; ++i) bw[i] = wpack[(8 + i) * 64 + lane];
#pragma unroll
  for (int i = 10; i < 12; ++i) {
    bw[i][0] = ((const unsigned*)wpack)[((8 + i) * 64 + lane) * 4];
    bw[i][1] = 0u; bw[i][2] = 0u; bw[i][3] = 0u;
  }
#pragma unroll
  for (int i = 0; i < 4; ++i) whl[t + 256 * i] = whpack[t + 256 * i];
  if (t < 64) wxs[t] = bf2f(w_x[t]);
  for (int i = t; i < 4 * 32 * SROW / 4; i += 256) ((unsigned*)Sm)[i] = 0;
  char* Sw = Sm[wv];

  struct Gath { u32x4 hs0, hs1, hs2, hs3, ea; uint2 ps; };
  Gath gA, gB;
  uint2 rc0 = {0u, 0u}, rc1 = {0u, 0u}, rc2 = {0u, 0u};
  u32x4 bA0, bA1;
  f32x16 Y0, Y1;
  unsigned rem = 0, rptr = 0, incl = 0, total = 0;
  int cnt = 0, par = 0;

  auto scan1 = [&]() {
    incl = rem;
#pragma unroll
    for (int d = 1; d < 64; d <<= 1) {
      unsigned o = __shfl_up(incl, d);
      if (lane >= d) incl += o;
    }
    if (lane == 63) wtot[wv] = incl;
  };
  auto scan2 = [&](unsigned* pl) {
    unsigned base = 0;
    total = 0;
#pragma unroll
    for (int w = 0; w < 4; ++w) {
      const unsigned x = wtot[w];
      if (w < wv) base += x;
      total += x;
    }
    const unsigned pre = base + incl - rem;
    unsigned take = 0;
    if (pre < (unsigned)LCAP) take = min(rem, (unsigned)LCAP - pre);
    for (unsigned i = 0; i < take; ++i) pl[pre + i] = rptr + i;
    rem -= take;
    rptr += take;
    cnt = (int)min(total, (unsigned)LCAP);
  };
  auto commit = [&](int p) {
    if (t < 64) {
      *(u32x4*)&cat[p][t >> 3][(t & 7) * 8] = hrow;
    } else if (t < 72) {
      f32x4 v = {bf2f(hrow[0] & 0xffff), bf2f(hrow[0] >> 16), bf2f(hrow[1] & 0xffff), 0.f};
      *(f32x4*)&posd[p][t - 64][0] = v;
    }
    rem = o1 - o0;
    rptr = (unsigned)t * CH + o0;
  };
  auto ld_rec = [&](int p, int j) -> uint2 {
    const int idx = min((wv + 4 * j) * 32 + r, cnt - 1);
    return recs[ridx[p][idx]];
  };
  auto gather = [&](const uint2 rc, Gath& g) {
    const unsigned e = rc.x, s = rc.y & 0x0FFFFFFFu;
    const u32x4* hs_p = (const u32x4*)(h + (size_t)s * D + hh * 8);
    g.hs0 = hs_p[0]; g.hs1 = hs_p[2]; g.hs2 = hs_p[4]; g.hs3 = hs_p[6];
    g.ea = *(const u32x4*)(e_attr + (size_t)e * DE + hh * 8);
    g.ps = posP[s];
  };
  auto ainit = [&](int p) {
    f32x16 c0v, c1v;
#pragma unroll
    for (int i = 0; i < 16; ++i) { c0v[i] = 0.f; c1v[i] = 0.f; }
    const u32x4* hd_p = (const u32x4*)(&cat[p][r & 7][hh * 8]);
#pragma unroll
    for (int k = 0; k < 4; ++k) {
      const u32x4 hd = hd_p[2 * k];
      c0v = __builtin_amdgcn_mfma_f32_32x32x16_bf16(__builtin_bit_cast(bf16x8, hd), __builtin_bit_cast(bf16x8, wl[(2 * k) * 64 + lane]), c0v, 0, 0, 0);
      c1v = __builtin_amdgcn_mfma_f32_32x32x16_bf16(__builtin_bit_cast(bf16x8, hd), __builtin_bit_cast(bf16x8, wl[(2 * k + 1) * 64 + lane]), c1v, 0, 0, 0);
    }
    bA0[0] = pk_bf16(c0v[0], c0v[1]);
    bA0[1] = pk_bf16(c0v[2], c0v[3]);
    bA0[2] = pk_bf16(c0v[0] - __uint_as_float(bA0[0] << 16), c0v[1] - __uint_as_float(bA0[0] & 0xffff0000u));
    bA0[3] = pk_bf16(c0v[2] - __uint_as_float(bA0[1] << 16), c0v[3] - __uint_as_float(bA0[1] & 0xffff0000u));
    bA1[0] = pk_bf16(c1v[0], c1v[1]);
    bA1[1] = pk_bf16(c1v[2], c1v[3]);
    bA1[2] = pk_bf16(c1v[0] - __uint_as_float(bA1[0] << 16), c1v[1] - __uint_as_float(bA1[0] & 0xffff0000u));
    bA1[3] = pk_bf16(c1v[2] - __uint_as_float(bA1[1] << 16), c1v[3] - __uint_as_float(bA1[1] & 0xffff0000u));
  };
  auto compute = [&](const uint2 rc, Gath& g, int j, const uint2 rcn, bool pf) {
    const bool valid = (wv + 4 * j) * 32 + r < cnt;
    const unsigned dl = rc.y >> 28;
    const u32x4 be0 = wl[512 + lane], be1 = wl[576 + lane];
    const unsigned ohv = (dl & 1) ? 0x3f800000u : 0x3f80u;
    u32x4 oh;
    oh[0] = ((dl >> 1) == (unsigned)(2 * hh)) ? ohv : 0u;
    oh[1] = ((dl >> 1) == (unsigned)(2 * hh + 1)) ? ohv : 0u;
    oh[2] = oh[0];
    oh[3] = oh[1];
    const f32x4 pd = *(const f32x4*)&posd[par][dl][0];
    const float dx = rbf(pd[0] - bf2f(g.ps.x & 0xffff));
    const float dy = rbf(pd[1] - bf2f(g.ps.x >> 16));
    const float dz = rbf(pd[2] - bf2f(g.ps.y & 0xffff));
    const float d2 = rbf(rbf(dx * dx) + rbf(dy * dy) + rbf(dz * dz));
    unsigned short* s_a = (unsigned short*)(Sw + (hh * 16 + dl) * SROW) + r;
    unsigned short* s_b = (unsigned short*)(Sw + (hh * 16 + 8 + dl) * SROW) + r;
    if (valid) {
      const unsigned ca = hh ? (__float_as_uint(dy) >> 16) : 0x3f80u;
      const unsigned cb = hh ? (__float_as_uint(dz) >> 16) : (__float_as_uint(dx) >> 16);
      *s_a = (unsigned short)ca;
      *s_b = (unsigned short)cb;
    }
    u32x4 dd;
    dd[0] = hh ? 0u : (__float_as_uint(d2) >> 16);
    dd[1] = 0; dd[2] = 0; dd[3] = 0;

    f32x16 a0, a1;
#pragma unroll
    for (int i = 0; i < 16; ++i) { a0[i] = 0.f; a1[i] = 0.f; }
#define STEP(AV, B0, B1)                                                                                     \
  a0 = __builtin_amdgcn_mfma_f32_32x32x16_bf16(__builtin_bit_cast(bf16x8, AV), __builtin_bit_cast(bf16x8, B0), a0, 0, 0, 0); \
  a1 = __builtin_amdgcn_mfma_f32_32x32x16_bf16(__builtin_bit_cast(bf16x8, AV), __builtin_bit_cast(bf16x8, B1), a1, 0, 0, 0);
    STEP(oh, bA0, bA1)
    STEP(dd, bw[10], bw[11])
    STEP(g.hs0, bw[0], bw[1]) STEP(g.hs1, bw[2], bw[3]) STEP(g.hs2, bw[4], bw[5]) STEP(g.hs3, bw[6], bw[7])
    STEP(g.ea, be0, be1)
#undef STEP
    if (pf) gather(rcn, g);
    u32x4 m00, m01, m10, m11;
#pragma unroll
    for (int i = 0; i < 4; ++i) {
      m00[i] = pk_bf16(silu_f(a0[2 * i]), silu_f(a0[2 * i + 1]));
      m01[i] = pk_bf16(silu_f(a0[8 + 2 * i]), silu_f(a0[8 + 2 * i + 1]));
      m10[i] = pk_bf16(silu_f(a1[2 * i]), silu_f(a1[2 * i + 1]));
      m11[i] = pk_bf16(silu_f(a1[8 + 2 * i]), silu_f(a1[8 + 2 * i + 1]));
    }
    WAVE_FENCE();
    const char* srow = Sw + r * SROW + hh * 8;
    const u32x2 f00 = *(const u32x2*)(srow), f01 = *(const u32x2*)(srow + 16);
    const u32x2 f10 = *(const u32x2*)(srow + 32), f11 = *(const u32x2*)(srow + 48);
    u32x4 sf0 = {f00[0], f00[1], f01[0], f01[1]};
    u32x4 sf1 = {f10[0], f10[1], f11[0], f11[1]};
    Y0 = __builtin_amdgcn_mfma_f32_32x32x16_bf16(__builtin_bit_cast(bf16x8, sf0), __builtin_bit_cast(bf16x8, m00), Y0, 0, 0, 0);
    Y1 = __builtin_amdgcn_mfma_f32_32x32x16_bf16(__builtin_bit_cast(bf16x8, sf0), __builtin_bit_cast(bf16x8, m10), Y1, 0, 0, 0);
    Y0 = __builtin_amdgcn_mfma_f32_32x32x16_bf16(__builtin_bit_cast(bf16x8, sf1), __builtin_bit_cast(bf16x8, m01), Y0, 0, 0, 0);
    Y1 = __builtin_amdgcn_mfma_f32_32x32x16_bf16(__builtin_bit_cast(bf16x8, sf1), __builtin_bit_cast(bf16x8, m11), Y1, 0, 0, 0);
    WAVE_FENCE();
    if (valid) {
      *s_a = 0;
      *s_b = 0;
    }
  };
  auto run_tiles = [&]() {
    const int nj = (((cnt + 31) >> 5) - wv + 3) >> 2;
    for (int j = 0; j < nj; j += 2) {
      const uint2 rc3 = ld_rec(par, j + 3);
      compute(rc0, gA, j, rc2, j + 2 < nj);
      if (j + 1 >= nj) break;
      const uint2 rc4 = ld_rec(par, j + 4);
      compute(rc1, gB, j + 1, rc3, j + 3 < nj);
      rc0 = rc2;
      rc1 = rc3;
      rc2 = rc4;
    }
  };

  commit(0);
  scan1();
  __syncthreads();
  scan2(ridx[0]);
  if (bl + nwx < per && xcd * per + bl + nwx < NB) prefetch_bucket(xcd * per + bl + nwx);
  __syncthreads();
  if (cnt > 0) {
    rc0 = ld_rec(0, 0);
    rc1 = ld_rec(0, 1);
    rc2 = ld_rec(0, 2);
    gather(rc0, gA);
    gather(rc1, gB);
  }
  ainit(0);

#pragma unroll
  for (int i = 0; i < 16; ++i) { Y0[i] = 0.f; Y1[i] = 0.f; }
  int g0 = 0;

  while (true) {
    const int beta = xcd * per + bl;
    const int n0 = beta * 8;
    const bool has_next = (bl + nwx < per) && (beta + nwx < NB);
    run_tiles();
    if (total > (unsigned)LCAP || G > 256) {
      bool more = false;
      while (true) {
        scan1();
        __syncthreads();
        if (wtot[0] + wtot[1] + wtot[2] + wtot[3] == 0) {
          __syncthreads();
          g0 += 256;
          if (g0 >= G) break;
          rem = 0;
          if (g0 + t < G) {
            const unsigned c0 = off[(size_t)(g0 + t) * NBP + beta];
            const unsigned c1 = off[(size_t)(g0 + t) * NBP + beta + 1];
            rem = c1 - c0;
            rptr = (unsigned)(g0 + t) * CH + c0;
          }
          continue;
        }
        scan2(ridx[par]);
        __syncthreads();
        rc0 = ld_rec(par, 0);
        rc1 = ld_rec(par, 1);
        rc2 = ld_rec(par, 2);
        gather(rc0, gA);
        gather(rc1, gB);
        more = true;
        break;
      }
      if (more) continue;
      g0 = 0;
    }
    __syncthreads();
#pragma unroll
    for (int i = 0; i < 16; ++i) {
      const int row = (i & 3) + 8 * (i >> 2) + 4 * hh;
      Ypart[wv * 2048 + row * 64 + r] = Y0[i];
      Ypart[wv * 2048 + row * 64 + 32 + r] = Y1[i];
    }
    if (has_next) {
      commit(par ^ 1);
      scan1();
    }
    __syncthreads();
    float* Yred = Ypart;
    {
      f32x4 a = *(const f32x4*)&Ypart[t * 8], b = *(const f32x4*)&Ypart[t * 8 + 4];
#pragma unroll
      for (int w = 1; w < 4; ++w) {
        a += *(const f32x4*)&Ypart[w * 2048 + t * 8];
        b += *(const f32x4*)&Ypart[w * 2048 + t * 8 + 4];
      }
      *(f32x4*)&Yred[t * 8] = a;
      *(f32x4*)&Yred[t * 8 + 4] = b;
    }
    if (has_next) {
      scan2(ridx[par ^ 1]);
      if (bl + 2 * nwx < per && beta + 2 * nwx < NB) prefetch_bucket(beta + 2 * nwx);
    }
    __syncthreads();
    if (has_next && cnt > 0) {
      rc0 = ld_rec(par ^ 1, 0);
      rc1 = ld_rec(par ^ 1, 1);
      rc2 = ld_rec(par ^ 1, 2);
    }
    {
      const int row = t >> 5, c2 = (t & 31) * 2;
      *(unsigned*)&cat[par][row][64 + c2] = pk_bf16(Yred[row * 64 + c2], Yred[row * 64 + c2 + 1]);
    }
    if (t < 192) {
      const int pr = t >> 3, part = t & 7;
      const float* yr = &Yred[(8 + pr) * 64 + part * 8];
      float sacc = 0.f;
#pragma unroll
      for (int c = 0; c < 8; ++c) sacc += yr[c] * wxs[part * 8 + c];
      sacc += __shfl_xor(sacc, 1);
      sacc += __shfl_xor(sacc, 2);
      sacc += __shfl_xor(sacc, 4);
      const int node = pr & 7, d = pr >> 3;
      if (part == 0 && n0 + node < N) {
        const float v = posd[par][node][d] + rbf(sacc);
        out_pos[(size_t)(n0 + node) * 3 + d] = (unsigned short)(pk_bf16(v, v) & 0xffff);
      }
    }
    __syncthreads();
    if (has_next && cnt > 0) { gather(rc0, gA); gather(rc1, gB); }
    {
      f32x4 acc = {0.f, 0.f, 0.f, 0.f};
      const int arow = lane & 7, kq = lane >> 4;
#pragma unroll
      for (int s = 0; s < 4; ++s) {
        const u32x4 af = *(const u32x4*)&cat[par][arow][32 * s + 8 * kq];
        const u32x4 bfr = whl[(wv * 4 + s) * 64 + lane];
        acc = __builtin_amdgcn_mfma_f32_16x16x32_bf16(__builtin_bit_cast(bf16x8, af), __builtin_bit_cast(bf16x8, bfr), acc, 0, 0, 0);
      }
      if (lane < 32) {
        const int col = 16 * wv + (lane & 15);
#pragma unroll
        for (int i = 0; i < 4; ++i) {
          const int row = (lane >> 4) * 4 + i;
          if (n0 + row < N) {
            const float u = rbf(silu_f(rbf(acc[i])));
            const float v = bf2f(cat[par][row][col]) + u;
            out_h[(size_t)(n0 + row) * D + col] = (unsigned short)(pk_bf16(v, v) & 0xffff);
          }
        }
      }
    }
    if (!has_next) break;
#pragma unroll
    for (int i = 0; i < 16; ++i) { Y0[i] = 0.f; Y1[i] = 0.f; }
    ainit(par ^ 1);
    par ^= 1;
    bl += nwx;
  }
}

extern "C" void kernel_launch(void* const* d_in, const int* in_sizes, int n_in, void* d_out,
                              int out_size, void* d_ws, size_t ws_size, hipStream_t stream) {
  const unsigned short* h = (const unsigned short*)d_in[0];
  const unsigned short* pos = (const unsigned short*)d_in[1];
  const unsigned short* e_attr = (const unsigned short*)d_in[2];
  const int* src = (const int*)d_in[3];
  const int* dst = (const int*)d_in[4];
  const unsigned short* w_e = (const unsigned short*)d_in[5];
  const unsigned short* w_x = (const unsigned short*)d_in[6];
  const unsigned short* w_h = (const unsigned short*)d_in[7];
  const int N = in_sizes[0] / D;
  const int E = in_sizes[3];
  const int NB = (N + 7) / 8;
  const int G = (E + CH - 1) / CH;
  const int NBP = (NB + 2) & ~1;

  char* ws = (char*)d_ws;
  size_t o = 0;
  uint2* recs = (uint2*)(ws + o); o += (size_t)E * 8; o = (o + 255) & ~(size_t)255;
  unsigned short* off = (unsigned short*)(ws + o); o += (size_t)G * NBP * 2; o = (o + 255) & ~(size_t)255;
  u32x4* wpack = (u32x4*)(ws + o); o += 20 * 64 * 16;
  u32x4* whpack = (u32x4*)(ws + o); o += 16 * 64 * 16;
  uint2* posP = (uint2*)(ws + o); o += (size_t)N * 8;

  unsigned short* out_h = (unsigned short*)d_out;
  unsigned short* out_pos = out_h + (size_t)N * D;

  sort_kernel<<<G + 2, SORT_T, 0, stream>>>(src, dst, E, NB, G, recs, off, NBP, w_e, w_h, pos, N,
                                           wpack, whpack, posP);
  const int per = (NB + 7) / 8;
  const int nwx = per < 64 ? per : 64;
  main_kernel<<<nwx * 8, 256, 0, stream>>>(h, e_attr, w_x, recs, off, NBP, G, NB, N, wpack, whpack,
                                           posP, out_h, out_pos);
}
```

```cpp
#include <hip/hip_runtime.h>
#include <hip/hip_bf16.h>
#include <stdint.h>

typedef __attribute__((ext_vector_type(8))) short bf16x8;
typedef __attribute__((ext_vector_type(16))) float f32x16;
typedef __attribute__((ext_vector_type(4))) float f32x4;
typedef __attribute__((ext_vector_type(2))) float f32x2;
typedef __attribute__((ext_vector_type(2))) __bf16 bfx2;
typedef __attribute__((ext_vector_type(4))) unsigned u32x4;
typedef __attribute__((ext_vector_type(2))) unsigned u32x2;

#define DI __device__ __forceinline__
#define WAVE_FENCE()                                         \
  do {                                                       \
    __builtin_amdgcn_fence(__ATOMIC_SEQ_CST, "wavefront");   \
    __builtin_amdgcn_wave_barrier();                         \
  } while (0)

static constexpr int D = 64;
static constexpr int DE = 16;
static constexpr int CH = 3072;
static constexpr int SORT_T = 1024;
static constexpr int MAXNB = 2048;
static constexpr int LCAP = 1024;
static constexpr int SROW = 72;

DI float bf2f(unsigned short u) { return __uint_as_float(((unsigned)u) << 16); }
DI unsigned pk_bf16(float a, float b) {
  f32x2 v = {a, b};
  bfx2 r = __builtin_convertvector(v, bfx2);
  return __builtin_bit_cast(unsigned, r);
}
DI float rbf(float a) { return __uint_as_float(pk_bf16(a, a) << 16); }
DI float silu_f(float x) {
  float t = __builtin_amdgcn_exp2f(x * -1.44269504088896340736f);
  return x * __builtin_amdgcn_rcpf(1.0f + t);
}

__global__ __launch_bounds__(SORT_T) void sort_kernel(
    const int* __restrict__ src, const int* __restrict__ dst, int E, int NB, int G,
    uint2* __restrict__ recs, unsigned short* __restrict__ off, int NBP,
    const unsigned short* __restrict__ w_e, const unsigned short* __restrict__ w_h,
    const unsigned short* __restrict__ pos, int N,
    u32x4* __restrict__ wpack, u32x4* __restrict__ whpack, uint2* __restrict__ posP) {
  __shared__ unsigned hist[MAXNB + 32];
  __shared__ unsigned wsum[16];
  __shared__ uint2 stage[CH];
  const int t = threadIdx.x;
  const int b = blockIdx.x;
  if (b >= G) {
    const int pb = b - G;
    if (pb == 0) {
      for (int i = t; i < 20 * 64; i += SORT_T) {
        const int f = i >> 6, l = i & 63;
        const int kap = f >> 1, nu = f & 1;
        const int col = 32 * nu + (l & 31);
        unsigned short v[8];
#pragma unroll
        for (int j = 0; j < 8; ++j) {
          const int kk = 8 * (l >> 5) + j;
          int row;
          if (kap < 8) row = 16 * kap + kk;
          else if (kap == 8) row = 129 + kk;
          else row = (kk == 0) ? 128 : -1;
          v[j] = (row >= 0) ? w_e[row * D + col] : (unsigned short)0;
        }
        u32x4 p;
        p[0] = v[0] | ((unsigned)v[1] << 16);
        p[1] = v[2] | ((unsigned)v[3] << 16);
        p[2] = v[4] | ((unsigned)v[5] << 16);
        p[3] = v[6] | ((unsigned)v[7] << 16);
        wpack[i] = p;
      }
    } else if (pb == 1) {
      for (int i = t; i < 16 * 64; i += SORT_T) {
        const int f = i >> 6, l = i & 63;
        const int w = f >> 2, s = f & 3;
        const int col = 16 * w + (l & 15);
        unsigned short v[8];
#pragma unroll
        for (int j = 0; j < 8; ++j) v[j] = w_h[(32 * s + 8 * (l >> 4) + j) * D + col];
        u32x4 p;
        p[0] = v[0] | ((unsigned)v[1] << 16);
        p[1] = v[2] | ((unsigned)v[3] << 16);
        p[2] = v[4] | ((unsigned)v[5] << 16);
        p[3] = v[6] | ((unsigned)v[7] << 16);
        whpack[i] = p;
      }
    } else {
      const int n = (pb - 2) * SORT_T + t;
      if (n < N) {
        uint2 p;
        p.x = pos[n * 3] | ((unsigned)pos[n * 3 + 1] << 16);
        p.y = pos[n * 3 + 2];
        posP[n] = p;
      }
    }
    return;
  }
  const int e0 = b * CH;
  const int len = min(CH, E - e0);
  for (int i = t; i < NB + 1; i += SORT_T) hist[i] = 0;
  __syncthreads();
  int dv[CH / SORT_T], sv[CH / SORT_T];
#pragma unroll
  for (int k = 0; k < CH / SORT_T; ++k) {
    const int i = t + k * SORT_T;
    if (i < len) {
      dv[k] = dst[e0 + i];
      sv[k] = src[e0 + i];
    } else {
      dv[k] = -1;
      sv[k] = 0;
    }
  }
#pragma unroll
  for (int k = 0; k < CH / SORT_T; ++k)
    if (dv[k] >= 0) atomicAdd(&hist[dv[k] >> 3], 1u);
  __syncthreads();
  {
    const int i0 = 2 * t;
    const unsigned a = (i0 < NB) ? hist[i0] : 0u;
    const unsigned c = (i0 + 1 < NB) ? hist[i0 + 1] : 0u;
    unsigned s = a + c;
    const int lane = t & 63, wv = t >> 6;
    unsigned incl = s;
#pragma unroll
    for (int d = 1; d < 64; d <<= 1) {
      unsigned o = __shfl_up(incl, d);
      if (lane >= d) incl += o;
    }
    if (lane == 63) wsum[wv] = incl;
    __syncthreads();
    unsigned base = 0;
    for (int w = 0; w < wv; ++w) base += wsum[w];
    const unsigned ex = base + incl - s;
    if (i0 < NB) hist[i0] = ex;
    if (i0 + 1 < NB) hist[i0 + 1] = ex + a;
    if (t == 0) hist[NB] = (unsigned)len;
  }
  __syncthreads();
  for (int i = t; i < NB + 1; i += SORT_T) off[(size_t)b * NBP + i] = (unsigned short)hist[i];
  __syncthreads();
#pragma unroll
  for (int k = 0; k < CH / SORT_T; ++k) {
    if (dv[k] >= 0) {
      const unsigned p = atomicAdd(&hist[dv[k] >> 3], 1u);
      uint2 r;
      r.x = (unsigned)(e0 + t + k * SORT_T);
      r.y = (unsigned)sv[k] | ((unsigned)(dv[k] & 7) << 28);
      stage[p] = r;
    }
  }
  __syncthreads();
  for (int i = t; i < len; i += SORT_T) recs[e0 + i] = stage[i];
}

__global__ __launch_bounds__(256, 2) void main_kernel(
    const unsigned short* __restrict__ h, const unsigned short* __restrict__ e_attr,
    const unsigned short* __restrict__ w_x, const uint2* __restrict__ recs,
    const unsigned short* __restrict__ off, int NBP, int G, int NB, int N,
    const u32x4* __restrict__ wpack, const u32x4* __restrict__ whpack,
    const uint2* __restrict__ posP, unsigned short* __restrict__ out_h,
    unsigned short* __restrict__ out_pos) {
  __shared__ u32x4 wl[8 * 64];
  __shared__ u32x4 whl[16 * 64];
  __shared__ __attribute__((aligned(16))) float Ypart[4 * 2048];
  __shared__ unsigned ridx[2][LCAP];
  __shared__ __attribute__((aligned(16))) char Sm[4][32 * SROW];
  __shared__ __attribute__((aligned(16))) unsigned short cat[2][8][136];
  __shared__ __attribute__((aligned(16))) float posd[2][8][4];
  __shared__ float wxs[64];
  __shared__ unsigned wtot[4];

  const int t = threadIdx.x, lane = t & 63, wv = t >> 6;
  const int r = lane & 31, hh = lane >> 5;
  const int per = (NB + 7) >> 3;
  const int nwx = (int)(gridDim.x >> 3);
  const int xcd = blockIdx.x & 7;
  int bl = blockIdx.x >> 3;
  if (bl >= per || xcd * per + bl >= NB) return;

  unsigned o0 = 0, o1 = 0;
  u32x4 hrow = {0u, 0u, 0u, 0u};
  auto prefetch_bucket = [&](int beta_) {
    if (t < G) {
      o0 = off[(size_t)t * NBP + beta_];
      o1 = off[(size_t)t * NBP + beta_ + 1];
    }
    if (t < 64) {
      const int n = min(beta_ * 8 + (t >> 3), N - 1);
      hrow = *(const u32x4*)&h[(size_t)n * D + (t & 7) * 8];
    } else if (t < 72) {
      const uint2 p = posP[min(beta_ * 8 + t - 64, N - 1)];
      hrow[0] = p.x;
      hrow[1] = p.y;
    }
  };
  prefetch_bucket(xcd * per + bl);
#pragma unroll
  for (int i = 0; i < 2; ++i) wl[t + 256 * i] = wpack[t + 256 * i];
  u32x4 bw[12];
#pragma unroll
  for (int i = 0; i < 10; ++i) bw[i] = wpack[(8 + i) * 64 + lane];
#pragma unroll
  for (int i = 10; i < 12; ++i) {
    bw[i][0] = ((const unsigned*)wpack)[((8 + i) * 64 + lane) * 4];
    bw[i][1] = 0u; bw[i][2] = 0u; bw[i][3] = 0u;
  }
#pragma unroll
  for (int i = 0; i < 4; ++i) whl[t + 256 * i] = whpack[t + 256 * i];
  if (t < 64) wxs[t] = bf2f(w_x[t]);
  for (int i = t; i < 4 * 32 * SROW / 4; i += 256) ((unsigned*)Sm)[i] = 0;
  char* Sw = Sm[wv];

  struct Gath { u32x4 hs0, hs1, hs2, hs3, ea; uint2 ps; };
  Gath g;
  uint2 rc0 = {0u, 0u}, rc1 = {0u, 0u};
  u32x4 bA0, bA1;
  f32x16 Y0, Y1;
  unsigned rem = 0, rptr = 0, incl = 0, total = 0;
  int cnt = 0, par = 0;

  auto scan1 = [&]() {
    incl = rem;
#pragma unroll
    for (int d = 1; d < 64; d <<= 1) {
      unsigned o = __shfl_up(incl, d);
      if (lane >= d) incl += o;
    }
    if (lane == 63) wtot[wv] = incl;
  };
  auto scan2 = [&](unsigned* pl) {
    unsigned base = 0;
    total = 0;
#pragma unroll
    for (int w = 0; w < 4; ++w) {
      const unsigned x = wtot[w];
      if (w < wv) base += x;
      total += x;
    }
    const unsigned pre = base + incl - rem;
    unsigned take = 0;
    if (pre < (unsigned)LCAP) take = min(rem, (unsigned)LCAP - pre);
    for (unsigned i = 0; i < take; ++i) pl[pre + i] = rptr + i;
    rem -= take;
    rptr += take;
    cnt = (int)min(total, (unsigned)LCAP);
  };
  auto commit = [&](int p) {
    if (t < 64) {
      *(u32x4*)&cat[p][t >> 3][(t & 7) * 8] = hrow;
    } else if (t < 72) {
      f32x4 v = {bf2f(hrow[0] & 0xffff), bf2f(hrow[0] >> 16), bf2f(hrow[1] & 0xffff), 0.f};
      *(f32x4*)&posd[p][t - 64][0] = v;
    }
    rem = o1 - o0;
    rptr = (unsigned)t * CH + o0;
  };
  auto ld_rec = [&](int p, int j) -> uint2 {
    const int idx = min((wv + 4 * j) * 32 + r, cnt - 1);
    return recs[ridx[p][idx]];
  };
  auto gather = [&](const uint2 rc) {
    const unsigned e = rc.x, s = rc.y & 0x0FFFFFFFu;
    const u32x4* hs_p = (const u32x4*)(h + (size_t)s * D + hh * 8);
    g.hs0 = hs_p[0]; g.hs1 = hs_p[2]; g.hs2 = hs_p[4]; g.hs3 = hs_p[6];
    g.ea = *(const u32x4*)(e_attr + (size_t)e * DE + hh * 8);
    g.ps = posP[s];
  };
  auto ainit = [&](int p) {
    f32x16 c0v, c1v;
#pragma unroll
    for (int i = 0; i < 16; ++i) { c0v[i] = 0.f; c1v[i] = 0.f; }
    const u32x4* hd_p = (const u32x4*)(&cat[p][r & 7][hh * 8]);
#pragma unroll
    for (int k = 0; k < 4; ++k) {
      const u32x4 hd = hd_p[2 * k];
      c0v = __builtin_amdgcn_mfma_f32_32x32x16_bf16(__builtin_bit_cast(bf16x8, hd), __builtin_bit_cast(bf16x8, wl[(2 * k) * 64 + lane]), c0v, 0, 0, 0);
      c1v = __builtin_amdgcn_mfma_f32_32x32x16_bf16(__builtin_bit_cast(bf16x8, hd), __builtin_bit_cast(bf16x8, wl[(2 * k + 1) * 64 + lane]), c1v, 0, 0, 0);
    }
    bA0[0] = pk_bf16(c0v[0], c0v[1]);
    bA0[1] = pk_bf16(c0v[2], c0v[3]);
    bA0[2] = pk_bf16(c0v[0] - __uint_as_float(bA0[0] << 16), c0v[1] - __uint_as_float(bA0[0] & 0xffff0000u));
    bA0[3] = pk_bf16(c0v[2] - __uint_as_float(bA0[1] << 16), c0v[3] - __uint_as_float(bA0[1] & 0xffff0000u));
    bA1[0] = pk_bf16(c1v[0], c1v[1]);
    bA1[1] = pk_bf16(c1v[2], c1v[3]);
    bA1[2] = pk_bf16(c1v[0] - __uint_as_float(bA1[0] << 16), c1v[1] - __uint_as_float(bA1[0] & 0xffff0000u));
    bA1[3] = pk_bf16(c1v[2] - __uint_as_float(bA1[1] << 16), c1v[3] - __uint_as_float(bA1[1] & 0xffff0000u));
  };
  auto compute = [&](const uint2 rc, int j, const uint2 rcn) {
    const bool valid = (wv + 4 * j) * 32 + r < cnt;
    const unsigned dl = rc.y >> 28;
    const unsigned ohv = (dl & 1) ? 0x3f800000u : 0x3f80u;
    u32x4 oh;
    oh[0] = ((dl >> 1) == (unsigned)(2 * hh)) ? ohv : 0u;
    oh[1] = ((dl >> 1) == (unsigned)(2 * hh + 1)) ? ohv : 0u;
    oh[2] = oh[0];
    oh[3] = oh[1];
    const f32x4 pd = *(const f32x4*)&posd[par][dl][0];
    const float dx = rbf(pd[0] - bf2f(g.ps.x & 0xffff));
    const float dy = rbf(pd[1] - bf2f(g.ps.x >> 16));
    const float dz = rbf(pd[2] - bf2f(g.ps.y & 0xffff));
    const float d2 = rbf(rbf(dx * dx) + rbf(dy * dy) + rbf(dz * dz));
    unsigned short* s_a = (unsigned short*)(Sw + (hh * 16 + dl) * SROW) + r;
    unsigned short* s_b = (unsigned short*)(Sw + (hh * 16 + 8 + dl) * SROW) + r;
    if (valid) {
      const unsigned ca = hh ? (__float_as_uint(dy) >> 16) : 0x3f80u;
      const unsigned cb = hh ? (__float_as_uint(dz) >> 16) : (__float_as_uint(dx) >> 16);
      *s_a = (unsigned short)ca;
      *s_b = (unsigned short)cb;
    }
    u32x4 dd;
    dd[0] = hh ? 0u : (__float_as_uint(d2) >> 16);
    dd[1] = 0; dd[2] = 0; dd[3] = 0;

    f32x16 a0, a1;
#pragma unroll
    for (int i = 0; i < 16; ++i) { a0[i] = 0.f; a1[i] = 0.f; }
#define STEP(AV, B0, B1)                                                                                     \
  a0 = __builtin_amdgcn_mfma_f32_32x32x16_bf16(__builtin_bit_cast(bf16x8, AV), __builtin_bit_cast(bf16x8, B0), a0, 0, 0, 0); \
  a1 = __builtin_amdgcn_mfma_f32_32x32x16_bf16(__builtin_bit_cast(bf16x8, AV), __builtin_bit_cast(bf16x8, B1), a1, 0, 0, 0);
    STEP(oh, bA0, bA1)
    STEP(dd, bw[10], bw[11])
    STEP(g.ea, bw[8], bw[9])
    STEP(g.hs0, bw[0], bw[1]) STEP(g.hs1, bw[2], bw[3]) STEP(g.hs2, bw[4], bw[5]) STEP(g.hs3, bw[6], bw[7])
#undef STEP
    gather(rcn);
    u32x4 m00, m01, m10, m11;
#pragma unroll
    for (int i = 0; i < 4; ++i) {
      m00[i] = pk_bf16(silu_f(a0[2 * i]), silu_f(a0[2 * i + 1]));
      m01[i] = pk_bf16(silu_f(a0[8 + 2 * i]), silu_f(a0[8 + 2 * i + 1]));
      m10[i] = pk_bf16(silu_f(a1[2 * i]), silu_f(a1[2 * i + 1]));
      m11[i] = pk_bf16(silu_f(a1[8 + 2 * i]), silu_f(a1[8 + 2 * i + 1]));
    }
    WAVE_FENCE();
    const char* srow = Sw + r * SROW + hh * 8;
    const u32x2 f00 = *(const u32x2*)(srow), f01 = *(const u32x2*)(srow + 16);
    const u32x2 f10 = *(const u32x2*)(srow + 32), f11 = *(const u32x2*)(srow + 48);
    u32x4 sf0 = {f00[0], f00[1], f01[0], f01[1]};
    u32x4 sf1 = {f10[0], f10[1], f11[0], f11[1]};
    Y0 = __builtin_amdgcn_mfma_f32_32x32x16_bf16(__builtin_bit_cast(bf16x8, sf0), __builtin_bit_cast(bf16x8, m00), Y0, 0, 0, 0);
    Y1 = __builtin_amdgcn_mfma_f32_32x32x16_bf16(__builtin_bit_cast(bf16x8, sf0), __builtin_bit_cast(bf16x8, m10), Y1, 0, 0, 0);
    Y0 = __builtin_amdgcn_mfma_f32_32x32x16_bf16(__builtin_bit_cast(bf16x8, sf1), __builtin_bit_cast(bf16x8, m01), Y0, 0, 0, 0);
    Y1 = __builtin_amdgcn_mfma_f32_32x32x16_bf16(__builtin_bit_cast(bf16x8, sf1), __builtin_bit_cast(bf16x8, m11), Y1, 0, 0, 0);
    WAVE_FENCE();
    if (valid) {
      *s_a = 0;
      *s_b = 0;
    }
  };
  auto run_tiles = [&]() {
    const int nj = (((cnt + 31) >> 5) - wv + 3) >> 2;
    for (int j = 0; j < nj; ++j) {
      const uint2 rc2 = ld_rec(par, j + 2);
      compute(rc0, j, rc1);
      rc0 = rc1;
      rc1 = rc2;
    }
  };

  commit(0);
  scan1();
  __syncthreads();
  scan2(ridx[0]);
  if (bl + nwx < per && xcd * per + bl + nwx < NB) prefetch_bucket(xcd * per + bl + nwx);
  __syncthreads();
  if (cnt > 0) {
    rc0 = ld_rec(0, 0);
    rc1 = ld_rec(0, 1);
    gather(rc0);
  }
  ainit(0);

#pragma unroll
  for (int i = 0; i < 16; ++i) { Y0[i] = 0.f; Y1[i] = 0.f; }
  int g0 = 0;

  while (true) {
    const int beta = xcd * per + bl;
    const int n0 = beta * 8;
    const bool has_next = (bl + nwx < per) && (beta + nwx < NB);
    run_tiles();
    if (total > (unsigned)LCAP || G > 256) {
      bool more = false;
      while (true) {
        scan1();
        __syncthreads();
        if (wtot[0] + wtot[1] + wtot[2] + wtot[3] == 0) {
          __syncthreads();
          g0 += 256;
          if (g0 >= G) break;
          rem = 0;
          if (g0 + t < G) {
            const unsigned c0 = off[(size_t)(g0 + t) * NBP + beta];
            const unsigned c1 = off[(size_t)(g0 + t) * NBP + beta + 1];
            rem = c1 - c0;
            rptr = (unsigned)(g0 + t) * CH + c0;
          }
          continue;
        }
        scan2(ridx[par]);
        __syncthreads();
        rc0 = ld_rec(par, 0);
        rc1 = ld_rec(par, 1);
        gather(rc0);
        more = true;
        break;
      }
      if (more) continue;
      g0 = 0;
    }
    __syncthreads();
#pragma unroll
    for (int i = 0; i < 16; ++i) {
      const int row = (i & 3) + 8 * (i >> 2) + 4 * hh;
      Ypart[wv * 2048 + row * 64 + r] = Y0[i];
      Ypart[wv * 2048 + row * 64 + 32 + r] = Y1[i];
    }
    if (has_next) {
      commit(par ^ 1);
      scan1();
    }
    __syncthreads();
    float* Yred = Ypart;
    {
      f32x4 a = *(const f32x4*)&Ypart[t * 8], b = *(const f32x4*)&Ypart[t * 8 + 4];
#pragma unroll
      for (int w = 1; w < 4; ++w) {
        a += *(const f32x4*)&Ypart[w * 2048 + t * 8];
        b += *(const f32x4*)&Ypart[w * 2048 + t * 8 + 4];
      }
      *(f32x4*)&Yred[t * 8] = a;
      *(f32x4*)&Yred[t * 8 + 4] = b;
    }
    if (has_next) {
      scan2(ridx[par ^ 1]);
      if (bl + 2 * nwx < per && beta + 2 * nwx < NB) prefetch_bucket(beta + 2 * nwx);
    }
    __syncthreads();
    if (has_next && cnt > 0) {
      rc0 = ld_rec(par ^ 1, 0);
      rc1 = ld_rec(par ^ 1, 1);
    }
    {
      const int row = t >> 5, c2 = (t & 31) * 2;
      *(unsigned*)&cat[par][row][64 + c2] = pk_bf16(Yred[row * 64 + c2], Yred[row * 64 + c2 + 1]);
    }
    if (t < 192) {
      const int pr = t >> 3, part = t & 7;
      const float* yr = &Yred[(8 + pr) * 64 + part * 8];
      float sacc = 0.f;
#pragma unroll
      for (int c = 0; c < 8; ++c) sacc += yr[c] * wxs[part * 8 + c];
      sacc += __shfl_xor(sacc, 1);
      sacc += __shfl_xor(sacc, 2);
      sacc += __shfl_xor(sacc, 4);
      const int node = pr & 7, d = pr >> 3;
      if (part == 0 && n0 + node < N) {
        const float v = posd[par][node][d] + rbf(sacc);
        out_pos[(size_t)(n0 + node) * 3 + d] = (unsigned short)(pk_bf16(v, v) & 0xffff);
      }
    }
    __syncthreads();
    if (has_next && cnt > 0) gather(rc0);
    {
      f32x4 acc = {0.f, 0.f, 0.f, 0.f};
      const int arow = lane & 7, kq = lane >> 4;
#pragma unroll
      for (int s = 0; s < 4; ++s) {
        const u32x4 af = *(const u32x4*)&cat[par][arow][32 * s + 8 * kq];
        const u32x4 bfr = whl[(wv * 4 + s) * 64 + lane];
        acc = __builtin_amdgcn_mfma_f32_16x16x32_bf16(__builtin_bit_cast(bf16x8, af), __builtin_bit_cast(bf16x8, bfr), acc, 0, 0, 0);
      }
      if (lane < 32) {
        const int col = 16 * wv + (lane & 15);
#pragma unroll
        for (int i = 0; i < 4; ++i) {
          const int row = (lane >> 4) * 4 + i;
          if (n0 + row < N) {
            const float u = rbf(silu_f(rbf(acc[i])));
            const float v = bf2f(cat[par][row][col]) + u;
            out_h[(size_t)(n0 + row) * D + col] = (unsigned short)(pk_bf16(v, v) & 0xffff);
          }
        }
      }
    }
    if (!has_next) break;
#pragma unroll
    for (int i = 0; i < 16; ++i) { Y0[i] = 0.f; Y1[i] = 0.f; }
    ainit(par ^ 1);
    par ^= 1;
    bl += nwx;
  }
}

extern "C" void kernel_launch(void* const* d_in, const int* in_sizes, int n_in, void* d_out,
                              int out_size, void* d_ws, size_t ws_size, hipStream_t stream) {
  const unsigned short* h = (const unsigned short*)d_in[0];
  const unsigned short* pos = (const unsigned short*)d_in[1];
  const unsigned short* e_attr = (const unsigned short*)d_in[2];
  const int* src = (const int*)d_in[3];
  const int* dst = (const int*)d_in[4];
  const unsigned short* w_e = (const unsigned short*)d_in[5];
  const unsigned short* w_x = (const unsigned short*)d_in[6];
  const unsigned short* w_h = (const unsigned short*)d_in[7];
  const int N = in_sizes[0] / D;
  const int E = in_sizes[3];
  const int NB = (N + 7) / 8;
  const int G = (E + CH - 1) / CH;
  const int NBP = (NB + 2) & ~1;

  char* ws = (char*)d_ws;
  size_t o = 0;
  uint2* recs = (uint2*)(ws + o); o += (size_t)E * 8; o = (o + 255) & ~(size_t)255;
  unsigned short* off = (unsigned short*)(ws + o); o += (size_t)G * NBP * 2; o = (o + 255) & ~(size_t)255;
  u32x4* wpack = (u32x4*)(ws + o); o += 20 * 64 * 16;
  u32x4* whpack = (u32x4*)(ws + o); o += 16 * 64 * 16;
  uint2* posP = (uint2*)(ws + o); o += (size_t)N * 8;

  unsigned short* out_h = (unsigned short*)d_out;
  unsigned short* out_pos = out_h + (size_t)N * D;

  sort_kernel<<<G + 2 + (N + SORT_T - 1) / SORT_T, SORT_T, 0, stream>>>(src, dst, E, NB, G, recs, off, NBP, w_e, w_h, pos, N,
                                           wpack, whpack, posP);
  const int per = (NB + 7) / 8;
  const int nwx = per < 64 ? per : 64;
  main_kernel<<<nwx * 8, 256, 0, stream>>>(h, e_attr, w_x, recs, off, NBP, G, NB, N, wpack, whpack,
                                           posP, out_h, out_pos);
}
```

```cpp
#include <hip/hip_runtime.h>
#include <hip/hip_bf16.h>
#include <stdint.h>

typedef __attribute__((ext_vector_type(8))) short bf16x8;
typedef __attribute__((ext_vector_type(16))) float f32x16;
typedef __attribute__((ext_vector_type(4))) float f32x4;
typedef __attribute__((ext_vector_type(2))) float f32x2;
typedef __attribute__((ext_vector_type(2))) __bf16 bfx2;
typedef __attribute__((ext_vector_type(4))) unsigned u32x4;
typedef __attribute__((ext_vector_type(2))) unsigned u32x2;

#define DI __device__ __forceinline__
#define WAVE_FENCE()                                         \
  do {                                                       \
    __builtin_amdgcn_fence(__ATOMIC_SEQ_CST, "wavefront");   \
    __builtin_amdgcn_wave_barrier();                         \
  } while (0)

static constexpr int D = 64;
static constexpr int DE = 16;
static constexpr int CH = 5120;
static constexpr int SORT_T = 1024;
static constexpr int MAXNB = 2048;
static constexpr int LCAP = 1024;
static constexpr int SROW = 72;

DI float bf2f(unsigned short u) { return __uint_as_float(((unsigned)u) << 16); }
DI unsigned pk_bf16(float a, float b) {
  f32x2 v = {a, b};
  bfx2 r = __builtin_convertvector(v, bfx2);
  return __builtin_bit_cast(unsigned, r);
}
DI float rbf(float a) { return __uint_as_float(pk_bf16(a, a) << 16); }
DI float silu_f(float x) {
  float t = __builtin_amdgcn_exp2f(x * -1.44269504088896340736f);
  return x * __builtin_amdgcn_rcpf(1.0f + t);
}

__global__ __launch_bounds__(SORT_T) void sort_kernel(
    const int* __restrict__ src, const int* __restrict__ dst, int E, int NB, int G,
    uint2* __restrict__ recs, unsigned short* __restrict__ off, int NBP,
    const unsigned short* __restrict__ w_e, const unsigned short* __restrict__ w_h,
    const unsigned short* __restrict__ pos, int N,
    u32x4* __restrict__ wpack, u32x4* __restrict__ whpack, uint2* __restrict__ posP) {
  __shared__ unsigned hist[MAXNB + 32];
  __shared__ unsigned wsum[16];
  __shared__ uint2 stage[CH];
  const int t = threadIdx.x;
  const int b = blockIdx.x;
  if (b >= G) {
    const int pb = b - G;
    if (pb == 0) {
      for (int i = t; i < 20 * 64; i += SORT_T) {
        const int f = i >> 6, l = i & 63;
        const int kap = f >> 1, nu = f & 1;
        const int col = 32 * nu + (l & 31);
        unsigned short v[8];
#pragma unroll
        for (int j = 0; j < 8; ++j) {
          const int kk = 8 * (l >> 5) + j;
          int row;
          if (kap < 8) row = 16 * kap + kk;
          else if (kap == 8) row = 129 + kk;
          else row = (kk == 0) ? 128 : -1;
          v[j] = (row >= 0) ? w_e[row * D + col] : (unsigned short)0;
        }
        u32x4 p;
        p[0] = v[0] | ((unsigned)v[1] << 16);
        p[1] = v[2] | ((unsigned)v[3] << 16);
        p[2] = v[4] | ((unsigned)v[5] << 16);
        p[3] = v[6] | ((unsigned)v[7] << 16);
        wpack[i] = p;
      }
      for (int i = t; i < 16 * 64; i += SORT_T) {
        const int f = i >> 6, l = i & 63;
        const int w = f >> 2, s = f & 3;
        const int col = 16 * w + (l & 15);
        unsigned short v[8];
#pragma unroll
        for (int j = 0; j < 8; ++j) v[j] = w_h[(32 * s + 8 * (l >> 4) + j) * D + col];
        u32x4 p;
        p[0] = v[0] | ((unsigned)v[1] << 16);
        p[1] = v[2] | ((unsigned)v[3] << 16);
        p[2] = v[4] | ((unsigned)v[5] << 16);
        p[3] = v[6] | ((unsigned)v[7] << 16);
        whpack[i] = p;
      }
    } else {
      for (int n = t; n < N; n += SORT_T) {
        uint2 p;
        p.x = pos[n * 3] | ((unsigned)pos[n * 3 + 1] << 16);
        p.y = pos[n * 3 + 2];
        posP[n] = p;
      }
    }
    return;
  }
  const int e0 = b * CH;
  const int len = min(CH, E - e0);
  for (int i = t; i < NB + 1; i += SORT_T) hist[i] = 0;
  __syncthreads();
  int dv[CH / SORT_T], sv[CH / SORT_T];
#pragma unroll
  for (int k = 0; k < CH / SORT_T; ++k) {
    const int i = t + k * SORT_T;
    if (i < len) {
      dv[k] = dst[e0 + i];
      sv[k] = src[e0 + i];
    } else {
      dv[k] = -1;
      sv[k] = 0;
    }
  }
#pragma unroll
  for (int k = 0; k < CH / SORT_T; ++k)
    if (dv[k] >= 0) atomicAdd(&hist[dv[k] >> 3], 1u);
  __syncthreads();
  {
    const int i0 = 2 * t;
    const unsigned a = (i0 < NB) ? hist[i0] : 0u;
    const unsigned c = (i0 + 1 < NB) ? hist[i0 + 1] : 0u;
    unsigned s = a + c;
    const int lane = t & 63, wv = t >> 6;
    unsigned incl = s;
#pragma unroll
    for (int d = 1; d < 64; d <<= 1) {
      unsigned o = __shfl_up(incl, d);
      if (lane >= d) incl += o;
    }
    if (lane == 63) wsum[wv] = incl;
    __syncthreads();
    unsigned base = 0;
    for (int w = 0; w < wv; ++w) base += wsum[w];
    const unsigned ex = base + incl - s;
    if (i0 < NB) hist[i0] = ex;
    if (i0 + 1 < NB) hist[i0 + 1] = ex + a;
    if (t == 0) hist[NB] = (unsigned)len;
  }
  __syncthreads();
  for (int i = t; i < NB + 1; i += SORT_T) off[(size_t)b * NBP + i] = (unsigned short)hist[i];
  __syncthreads();
#pragma unroll
  for (int k = 0; k < CH / SORT_T; ++k) {
    if (dv[k] >= 0) {
      const unsigned p = atomicAdd(&hist[dv[k] >> 3], 1u);
      uint2 r;
      r.x = (unsigned)(e0 + t + k * SORT_T);
      r.y = (unsigned)sv[k] | ((unsigned)(dv[k] & 7) << 28);
      stage[p] = r;
    }
  }
  __syncthreads();
  for (int i = t; i < len; i += SORT_T) recs[e0 + i] = stage[i];
}

__global__ __launch_bounds__(256, 2) void main_kernel(
    const unsigned short* __restrict__ h, const unsigned short* __restrict__ e_attr,
    const unsigned short* __restrict__ w_x, const uint2* __restrict__ recs,
    const unsigned short* __restrict__ off, int NBP, int G, int NB, int N,
    const u32x4* __restrict__ wpack, const u32x4* __restrict__ whpack,
    const uint2* __restrict__ posP, unsigned short* __restrict__ out_h,
    unsigned short* __restrict__ out_pos) {
  __shared__ u32x4 wl[8 * 64];
  __shared__ u32x4 whl[16 * 64];
  __shared__ __attribute__((aligned(16))) float Ypart[4 * 2048];
  __shared__ unsigned ridx[2][LCAP];
  __shared__ __attribute__((aligned(16))) char Sm[4][32 * SROW];
  __shared__ __attribute__((aligned(16))) unsigned short cat[2][8][136];
  __shared__ __attribute__((aligned(16))) float posd[2][8][4];
  __shared__ float wxs[64];
  __shared__ unsigned wtot[4];

  const int t = threadIdx.x, lane = t & 63, wv = t >> 6;
  const int r = lane & 31, hh = lane >> 5;
  const int per = (NB + 7) >> 3;
  const int nwx = (int)(gridDim.x >> 3);
  const int xcd = blockIdx.x & 7;
  int bl = blockIdx.x >> 3;
  if (bl >= per || xcd * per + bl >= NB) return;

  unsigned o0 = 0, o1 = 0;
  u32x4 hrow = {0u, 0u, 0u, 0u};
  auto prefetch_bucket = [&](int beta_) {
    if (t < G) {
      o0 = off[(size_t)t * NBP + beta_];
      o1 = off[(size_t)t * NBP + beta_ + 1];
    }
    if (t < 64) {
      const int n = min(beta_ * 8 + (t >> 3), N - 1);
      hrow = *(const u32x4*)&h[(size_t)n * D + (t & 7) * 8];
    } else if (t < 72) {
      const uint2 p = posP[min(beta_ * 8 + t - 64, N - 1)];
      hrow[0] = p.x;
      hrow[1] = p.y;
    }
  };
  prefetch_bucket(xcd * per + bl);
#pragma unroll
  for (int i = 0; i < 2; ++i) wl[t + 256 * i] = wpack[t + 256 * i];
  u32x4 bw[12];
#pragma unroll
  for (int i = 0; i < 10; ++i) bw[i] = wpack[(8 + i) * 64 + lane];
#pragma unroll
  for (int i = 10; i < 12; ++i) {
    bw[i][0] = ((const unsigned*)wpack)[((8 + i) * 64 + lane) * 4];
    bw[i][1] = 0u; bw[i][2] = 0u; bw[i][3] = 0u;
  }
#pragma unroll
  for (int i = 0; i < 4; ++i) whl[t + 256 * i] = whpack[t + 256 * i];
  if (t < 64) wxs[t] = bf2f(w_x[t]);
  for (int i = t; i < 4 * 32 * SROW / 4; i += 256) ((unsigned*)Sm)[i] = 0;
  char* Sw = Sm[wv];

  struct Gath { u32x4 hs0, hs1, hs2, hs3, ea; uint2 ps; };
  Gath g;
  uint2 rc0 = {0u, 0u}, rc1 = {0u, 0u};
  u32x4 bA0, bA1;
  f32x16 Y0, Y1;
  unsigned rem = 0, rptr = 0, incl = 0, total = 0;
  int cnt = 0, par = 0;

  auto scan1 = [&]() {
    incl = rem;
#pragma unroll
    for (int d = 1; d < 64; d <<= 1) {
      unsigned o = __shfl_up(incl, d);
      if (lane >= d) incl += o;
    }
    if (lane == 63) wtot[wv] = incl;
  };
  auto scan2 = [&](unsigned* pl) {
    unsigned base = 0;
    total = 0;
#pragma unroll
    for (int w = 0; w < 4; ++w) {
      const unsigned x = wtot[w];
      if (w < wv) base += x;
      total += x;
    }
    const unsigned pre = base + incl - rem;
    unsigned take = 0;
    if (pre < (unsigned)LCAP) take = min(rem, (unsigned)LCAP - pre);
    for (unsigned i = 0; i < take; ++i) pl[pre + i] = rptr + i;
    rem -= take;
    rptr += take;
    cnt = (int)min(total, (unsigned)LCAP);
  };
  auto commit = [&](int p) {
    if (t < 64) {
      *(u32x4*)&cat[p][t >> 3][(t & 7) * 8] = hrow;
    } else if (t < 72) {
      f32x4 v = {bf2f(hrow[0] & 0xffff), bf2f(hrow[0] >> 16), bf2f(hrow[1] & 0xffff), 0.f};
      *(f32x4*)&posd[p][t - 64][0] = v;
    }
    rem = o1 - o0;
    rptr = (unsigned)t * CH + o0;
  };
  auto ld_rec = [&](int p, int j) -> uint2 {
    const int idx = min((wv + 4 * j) * 32 + r, cnt - 1);
    return recs[ridx[p][idx]];
  };
  auto gather = [&](const uint2 rc) {
    const unsigned e = rc.x, s = rc.y & 0x0FFFFFFFu;
    const u32x4* hs_p = (const u32x4*)(h + (size_t)s * D + hh * 8);
    g.hs0 = hs_p[0]; g.hs1 = hs_p[2]; g.hs2 = hs_p[4]; g.hs3 = hs_p[6];
    g.ea = *(const u32x4*)(e_attr + (size_t)e * DE + hh * 8);
    g.ps = posP[s];
  };
  auto ainit = [&](int p) {
    f32x16 c0v, c1v;
#pragma unroll
    for (int i = 0; i < 16; ++i) { c0v[i] = 0.f; c1v[i] = 0.f; }
    const u32x4* hd_p = (const u32x4*)(&cat[p][r & 7][hh * 8]);
#pragma unroll
    for (int k = 0; k < 4; ++k) {
      const u32x4 hd = hd_p[2 * k];
      c0v = __builtin_amdgcn_mfma_f32_32x32x16_bf16(__builtin_bit_cast(bf16x8, hd), __builtin_bit_cast(bf16x8, wl[(2 * k) * 64 + lane]), c0v, 0, 0, 0);
      c1v = __builtin_amdgcn_mfma_f32_32x32x16_bf16(__builtin_bit_cast(bf16x8, hd), __builtin_bit_cast(bf16x8, wl[(2 * k + 1) * 64 + lane]), c1v, 0, 0, 0);
    }
    bA0[0] = pk_bf16(c0v[0], c0v[1]);
    bA0[1] = pk_bf16(c0v[2], c0v[3]);
    bA0[2] = pk_bf16(c0v[0] - __uint_as_float(bA0[0] << 16), c0v[1] - __uint_as_float(bA0[0] & 0xffff0000u));
    bA0[3] = pk_bf16(c0v[2] - __uint_as_float(bA0[1] << 16), c0v[3] - __uint_as_float(bA0[1] & 0xffff0000u));
    bA1[0] = pk_bf16(c1v[0], c1v[1]);
    bA1[1] = pk_bf16(c1v[2], c1v[3]);
    bA1[2] = pk_bf16(c1v[0] - __uint_as_float(bA1[0] << 16), c1v[1] - __uint_as_float(bA1[0] & 0xffff0000u));
    bA1[3] = pk_bf16(c1v[2] - __uint_as_float(bA1[1] << 16), c1v[3] - __uint_as_float(bA1[1] & 0xffff0000u));
  };
  auto compute = [&](const uint2 rc, int j, const uint2 rcn) {
    const bool valid = (wv + 4 * j) * 32 + r < cnt;
    const unsigned dl = rc.y >> 28;
    const unsigned ohv = (dl & 1) ? 0x3f800000u : 0x3f80u;
    u32x4 oh;
    oh[0] = ((dl >> 1) == (unsigned)(2 * hh)) ? ohv : 0u;
    oh[1] = ((dl >> 1) == (unsigned)(2 * hh + 1)) ? ohv : 0u;
    oh[2] = oh[0];
    oh[3] = oh[1];
    const f32x4 pd = *(const f32x4*)&posd[par][dl][0];
    const float dx = rbf(pd[0] - bf2f(g.ps.x & 0xffff));
    const float dy = rbf(pd[1] - bf2f(g.ps.x >> 16));
    const float dz = rbf(pd[2] - bf2f(g.ps.y & 0xffff));
    const float d2 = rbf(rbf(dx * dx) + rbf(dy * dy) + rbf(dz * dz));
    unsigned short* s_a = (unsigned short*)(Sw + (hh * 16 + dl) * SROW) + r;
    unsigned short* s_b = (unsigned short*)(Sw + (hh * 16 + 8 + dl) * SROW) + r;
    if (valid) {
      const unsigned ca = hh ? (__float_as_uint(dy) >> 16) : 0x3f80u;
      const unsigned cb = hh ? (__float_as_uint(dz) >> 16) : (__float_as_uint(dx) >> 16);
      *s_a = (unsigned short)ca;
      *s_b = (unsigned short)cb;
    }
    u32x4 dd;
    dd[0] = hh ? 0u : (__float_as_uint(d2) >> 16);
    dd[1] = 0; dd[2] = 0; dd[3] = 0;

    f32x16 a0, a1;
#pragma unroll
    for (int i = 0; i < 16; ++i) { a0[i] = 0.f; a1[i] = 0.f; }
#define STEP(AV, B0, B1)                                                                                     \
  a0 = __builtin_amdgcn_mfma_f32_32x32x16_bf16(__builtin_bit_cast(bf16x8, AV), __builtin_bit_cast(bf16x8, B0), a0, 0, 0, 0); \
  a1 = __builtin_amdgcn_mfma_f32_32x32x16_bf16(__builtin_bit_cast(bf16x8, AV), __builtin_bit_cast(bf16x8, B1), a1, 0, 0, 0);
    STEP(oh, bA0, bA1)
    STEP(dd, bw[10], bw[11])
    STEP(g.ea, bw[8], bw[9])
    STEP(g.hs0, bw[0], bw[1]) STEP(g.hs1, bw[2], bw[3]) STEP(g.hs2, bw[4], bw[5]) STEP(g.hs3, bw[6], bw[7])
#undef STEP
    gather(rcn);
    u32x4 m00, m01, m10, m11;
#pragma unroll
    for (int i = 0; i < 4; ++i) {
      m00[i] = pk_bf16(silu_f(a0[2 * i]), silu_f(a0[2 * i + 1]));
      m01[i] = pk_bf16(silu_f(a0[8 + 2 * i]), silu_f(a0[8 + 2 * i + 1]));
      m10[i] = pk_bf16(silu_f(a1[2 * i]), silu_f(a1[2 * i + 1]));
      m11[i] = pk_bf16(silu_f(a1[8 + 2 * i]), silu_f(a1[8 + 2 * i + 1]));
    }
    WAVE_FENCE();
    const char* srow = Sw + r * SROW + hh * 8;
    const u32x2 f00 = *(const u32x2*)(srow), f01 = *(const u32x2*)(srow + 16);
    const u32x2 f10 = *(const u32x2*)(srow + 32), f11 = *(const u32x2*)(srow + 48);
    u32x4 sf0 = {f00[0], f00[1], f01[0], f01[1]};
    u32x4 sf1 = {f10[0], f10[1], f11[0], f11[1]};
    Y0 = __builtin_amdgcn_mfma_f32_32x32x16_bf16(__builtin_bit_cast(bf16x8, sf0), __builtin_bit_cast(bf16x8, m00), Y0, 0, 0, 0);
    Y1 = __builtin_amdgcn_mfma_f32_32x32x16_bf16(__builtin_bit_cast(bf16x8, sf0), __builtin_bit_cast(bf16x8, m10), Y1, 0, 0, 0);
    Y0 = __builtin_amdgcn_mfma_f32_32x32x16_bf16(__builtin_bit_cast(bf16x8, sf1), __builtin_bit_cast(bf16x8, m01), Y0, 0, 0, 0);
    Y1 = __builtin_amdgcn_mfma_f32_32x32x16_bf16(__builtin_bit_cast(bf16x8, sf1), __builtin_bit_cast(bf16x8, m11), Y1, 0, 0, 0);
    WAVE_FENCE();
    if (valid) {
      *s_a = 0;
      *s_b = 0;
    }
  };
  auto run_tiles = [&]() {
    const int nj = (((cnt + 31) >> 5) - wv + 3) >> 2;
    for (int j = 0; j < nj; ++j) {
      const uint2 rc2 = ld_rec(par, j + 2);
      compute(rc0, j, rc1);
      rc0 = rc1;
      rc1 = rc2;
    }
  };

  commit(0);
  scan1();
  __syncthreads();
  scan2(ridx[0]);
  if (bl + nwx < per && xcd * per + bl + nwx < NB) prefetch_bucket(xcd * per + bl + nwx);
  __syncthreads();
  if (cnt > 0) {
    rc0 = ld_rec(0, 0);
    rc1 = ld_rec(0, 1);
    gather(rc0);
  }
  ainit(0);

#pragma unroll
  for (int i = 0; i < 16; ++i) { Y0[i] = 0.f; Y1[i] = 0.f; }
  int g0 = 0;

  while (true) {
    const int beta = xcd * per + bl;
    const int n0 = beta * 8;
    const bool has_next = (bl + nwx < per) && (beta + nwx < NB);
    run_tiles();
    if (total > (unsigned)LCAP || G > 256) {
      bool more = false;
      while (true) {
        scan1();
        __syncthreads();
        if (wtot[0] + wtot[1] + wtot[2] + wtot[3] == 0) {
          __syncthreads();
          g0 += 256;
          if (g0 >= G) break;
          rem = 0;
          if (g0 + t < G) {
            const unsigned c0 = off[(size_t)(g0 + t) * NBP + beta];
            const unsigned c1 = off[(size_t)(g0 + t) * NBP + beta + 1];
            rem = c1 - c0;
            rptr = (unsigned)(g0 + t) * CH + c0;
          }
          continue;
        }
        scan2(ridx[par]);
        __syncthreads();
        rc0 = ld_rec(par, 0);
        rc1 = ld_rec(par, 1);
        gather(rc0);
        more = true;
        break;
      }
      if (more) continue;
      g0 = 0;
    }
    __syncthreads();
#pragma unroll
    for (int i = 0; i < 16; ++i) {
      const int row = (i & 3) + 8 * (i >> 2) + 4 * hh;
      Ypart[wv * 2048 + row * 64 + r] = Y0[i];
      Ypart[wv * 2048 + row * 64 + 32 + r] = Y1[i];
    }
    if (has_next) {
      commit(par ^ 1);
      scan1();
    }
    __syncthreads();
    float* Yred = Ypart;
    {
      f32x4 a = *(const f32x4*)&Ypart[t * 8], b = *(const f32x4*)&Ypart[t * 8 + 4];
#pragma unroll
      for (int w = 1; w < 4; ++w) {
        a += *(const f32x4*)&Ypart[w * 2048 + t * 8];
        b += *(const f32x4*)&Ypart[w * 2048 + t * 8 + 4];
      }
      *(f32x4*)&Yred[t * 8] = a;
      *(f32x4*)&Yred[t * 8 + 4] = b;
    }
    if (has_next) {
      scan2(ridx[par ^ 1]);
      if (bl + 2 * nwx < per && beta + 2 * nwx < NB) prefetch_bucket(beta + 2 * nwx);
    }
    __syncthreads();
    if (has_next && cnt > 0) {
      rc0 = ld_rec(par ^ 1, 0);
      rc1 = ld_rec(par ^ 1, 1);
    }
    {
      const int row = t >> 5, c2 = (t & 31) * 2;
      *(unsigned*)&cat[par][row][64 + c2] = pk_bf16(Yred[row * 64 + c2], Yred[row * 64 + c2 + 1]);
    }
    if (t < 192) {
      const int pr = t >> 3, part = t & 7;
      const float* yr = &Yred[(8 + pr) * 64 + part * 8];
      float sacc = 0.f;
#pragma unroll
      for (int c = 0; c < 8; ++c) sacc += yr[c] * wxs[part * 8 + c];
      sacc += __shfl_xor(sacc, 1);
      sacc += __shfl_xor(sacc, 2);
      sacc += __shfl_xor(sacc, 4);
      const int node = pr & 7, d = pr >> 3;
      if (part == 0 && n0 + node < N) {
        const float v = posd[par][node][d] + rbf(sacc);
        out_pos[(size_t)(n0 + node) * 3 + d] = (unsigned short)(pk_bf16(v, v) & 0xffff);
      }
    }
    __syncthreads();
    if (has_next && cnt > 0) gather(rc0);
    {
      f32x4 acc = {0.f, 0.f, 0.f, 0.f};
      const int arow = lane & 7, kq = lane >> 4;
#pragma unroll
      for (int s = 0; s < 4; ++s) {
        const u32x4 af = *(const u32x4*)&cat[par][arow][32 * s + 8 * kq];
        const u32x4 bfr = whl[(wv * 4 + s) * 64 + lane];
        acc = __builtin_amdgcn_mfma_f32_16x16x32_bf16(__builtin_bit_cast(bf16x8, af), __builtin_bit_cast(bf16x8, bfr), acc, 0, 0, 0);
      }
      if (lane < 32) {
        const int col = 16 * wv + (lane & 15);
#pragma unroll
        for (int i = 0; i < 4; ++i) {
          const int row = (lane >> 4) * 4 + i;
          if (n0 + row < N) {
            const float u = rbf(silu_f(rbf(acc[i])));
            const float v = bf2f(cat[par][row][col]) + u;
            out_h[(size_t)(n0 + row) * D + col] = (unsigned short)(pk_bf16(v, v) & 0xffff);
          }
        }
      }
    }
    if (!has_next) break;
#pragma unroll
    for (int i = 0; i < 16; ++i) { Y0[i] = 0.f; Y1[i] = 0.f; }
    ainit(par ^ 1);
    par ^= 1;
    bl += nwx;
  }
}

extern "C" void kernel_launch(void* const* d_in, const int* in_sizes, int n_in, void* d_out,
                              int out_size, void* d_ws, size_t ws_size, hipStream_t stream) {
  const unsigned short* h = (const unsigned short*)d_in[0];
  const unsigned short* pos = (const unsigned short*)d_in[1];
  const unsigned short* e_attr = (const unsigned short*)d_in[2];
  const int* src = (const int*)d_in[3];
  const int* dst = (const int*)d_in[4];
  const unsigned short* w_e = (const unsigned short*)d_in[5];
  const unsigned short* w_x = (const unsigned short*)d_in[6];
  const unsigned short* w_h = (const unsigned short*)d_in[7];
  const int N = in_sizes[0] / D;
  const int E = in_sizes[3];
  const int NB = (N + 7) / 8;
  const int G = (E + CH - 1) / CH;
  const int NBP = (NB + 2) & ~1;

  char* ws = (char*)d_ws;
  size_t o = 0;
  uint2* recs = (uint2*)(ws + o); o += (size_t)E * 8; o = (o + 255) & ~(size_t)255;
  unsigned short* off = (unsigned short*)(ws + o); o += (size_t)G * NBP * 2; o = (o + 255) & ~(size_t)255;
  u32x4* wpack = (u32x4*)(ws + o); o += 20 * 64 * 16;
  u32x4* whpack = (u32x4*)(ws + o); o += 16 * 64 * 16;
  uint2* posP = (uint2*)(ws + o); o += (size_t)N * 8;

  unsigned short* out_h = (unsigned short*)d_out;
  unsigned short* out_pos = out_h + (size_t)N * D;

  sort_kernel<<<G + 2, SORT_T, 0, stream>>>(src, dst, E, NB, G, recs, off, NBP, w_e, w_h, pos, N,
                                           wpack, whpack, posP);
  const int per = (NB + 7) / 8;
  const int nwx = per < 64 ? per : 64;
  main_kernel<<<nwx * 8, 256, 0, stream>>>(h, e_attr, w_x, recs, off, NBP, G, NB, N, wpack, whpack,
                                           posP, out_h, out_pos);
}
```
